```python
import torch

def _softmax(x, axis=-1):
    return torch.softmax(x, dim=axis)

def _gelu(x):
    return torch.nn.functional.gelu(x, approximate='tanh')

def _silu(x):
    return torch.nn.functional.silu(x)

def _sigmoid(x):
    return torch.sigmoid(x)

def _layernorm(x, axis=-1, eps=1e-05):
    m = x.mean(dim=axis, keepdim=True)
    v = x.var(dim=axis, keepdim=True, unbiased=False)
    return (x - m) / torch.sqrt(v + eps)

def _scatter_add(tgt, idx, src, axis=0):
    return tgt.clone().index_add_(axis, idx, src)

def _segment_sum(src, seg, n):
    out = torch.zeros((n,) + src.shape[1:], dtype=src.dtype)
    return out.index_add_(0, seg, src)

def _pad(x, pw):
    flat = tuple((v for pair in reversed(pw) for v in pair))
    return torch.nn.functional.pad(x, flat)
kernel_name = 'adaLN_cond__gqa_block_cached__dense_activation_silu__decode'

def setup_inputs():

    _w = lambda a: (lambda t: t.to(torch.bfloat16) if t.is_floating_point() else t)(torch.as_tensor(a))

    def _A_setup():
        B, Sq, D = (2, 1, 512)
        Hq, Hkv, Sk, Dh = (32, 8, 32768, 128)
        assert Hq % Hkv == 0, (Hq, Hkv)
        return {'x': _w((lambda *s: torch.randn(*s, dtype=torch.bfloat16) * 0.1)(B, Sq, D)), 'k_cache': _w((lambda *s: torch.randn(*s, dtype=torch.bfloat16) * 0.8)(B, Hkv, Sk, Dh)), 'v_cache': _w((lambda *s: torch.randn(*s, dtype=torch.bfloat16) * 3.2)(B, Hkv, Sk, Dh)), 'wq': _w((lambda *s: torch.randn(*s, dtype=torch.bfloat16) * 0.8)(D, Hq * Dh) * D ** (-0.5)), 'wo': _w((lambda *s: torch.randn(*s, dtype=torch.bfloat16) * 3.2)(Hq * Dh, D) * (Hq * Dh) ** (-0.5))}
    _A_in = _A_setup()

    def _B_setup():
        B, S, D, F = (2, 1, 512, 1792)
        return {'x': _w((lambda *s: torch.randn(*s, dtype=torch.bfloat16) * 0.1)(B, S, D)), 'w1': _w((lambda *s: torch.randn(*s, dtype=torch.bfloat16) * 3.2)(D, F) * D ** (-0.5)), 'w2': _w((lambda *s: torch.randn(*s, dtype=torch.bfloat16) * 3.2)(F, D) * F ** (-0.5))}
    _B_in = _B_setup()
    return {'A_x': _A_in['x'], 'A_k_cache': _A_in['k_cache'], 'A_v_cache': _A_in['v_cache'], 'A_wq': _A_in['wq'], 'A_wo': _A_in['wo'], 'B_x': _B_in['x'], 'B_w1': _B_in['w1'], 'B_w2': _B_in['w2'], 'c': _w((lambda *s: torch.randn(*s, dtype=torch.bfloat16) * 0.4)(2, 512) * 8.0), 'w_mod': _w((lambda *s: torch.randn(*s, dtype=torch.bfloat16) * 0.4)(512, 6 * 512) * 512 ** (-0.5) * 8.0)}

def reference(A_x, A_k_cache, A_v_cache, A_wq, A_wo, B_x, B_w1, B_w2, c, w_mod):

    def _A_ref(x, k_cache, v_cache, wq, wo):
        B, Sq, D = x.shape
        Hq, Dh = (32, 128)
        Hkv = k_cache.shape[1]
        rep = Hq // Hkv
        q = (lambda x, *d: x.permute(*d))((x @ wq).reshape(B, Sq, Hq, Dh), 0, 2, 1, 3)
        k = (lambda x, n, axis: torch.repeat_interleave(x, n, dim=axis))(k_cache, rep, axis=1)
        v = (lambda x, n, axis: torch.repeat_interleave(x, n, dim=axis))(v_cache, rep, axis=1)
        scores = torch.einsum('bhqd,bhkd->bhqk', q, k) * Dh ** (-0.5)
        p = _softmax(scores, axis=-1)
        o = torch.einsum('bhqk,bhkd->bhqd', p, v)
        return (lambda x, *d: x.permute(*d))(o, 0, 2, 1, 3).reshape(B, Sq, Hq * Dh) @ wo

    def _B_ref(x, w1, w2):
        h = torch.einsum('bsd,df->bsf', x, w1)
        h = _silu(h)
        out = torch.einsum('bsf,fd->bsd', h, w2)
        return out
    x = A_x
    mod = (_silu(c) @ w_mod).reshape(c.shape[0], 6, 1, -1)
    sa, ca, ga, sb, cb, gb = (mod[:, 0].clone(), mod[:, 1].clone(), mod[:, 2].clone(), mod[:, 3].clone(), mod[:, 4].clone(), mod[:, 5].clone())
    h = _layernorm(x, axis=-1) * (1 + ca) + sa
    x = x + ga * _A_ref(h, A_k_cache, A_v_cache, A_wq, A_wo)
    h = _layernorm(x, axis=-1) * (1 + cb) + sb
    x = x + gb * _B_ref(h, B_w1, B_w2)
    return x
```

```cpp
#include <hip/hip_runtime.h>
#include <hip/hip_bf16.h>
#include <stdint.h>

typedef unsigned short u16;
typedef unsigned v4u __attribute__((ext_vector_type(4)));
typedef unsigned v2u __attribute__((ext_vector_type(2)));
typedef float f32x4 __attribute__((ext_vector_type(4)));
typedef short bf16x8 __attribute__((ext_vector_type(8)));
typedef __bf16 bf16x2_t __attribute__((ext_vector_type(2)));
#define GAS __attribute__((address_space(1)))

constexpr int B = 2, D = 512, HQ = 32, HKV = 8, SK = 32768, DH = 128, F = 1792;
constexpr int NSPLIT = 16;
constexpr int KEYS_PER_WG = SK / NSPLIT;
constexpr int NW = 8;

__device__ __forceinline__ float bf2f(u16 b) { return __builtin_bit_cast(float, (unsigned)b << 16); }
__device__ __forceinline__ float lo2f(unsigned u) { return __builtin_bit_cast(float, u << 16); }
__device__ __forceinline__ float hi2f(unsigned u) { return __builtin_bit_cast(float, u & 0xffff0000u); }
__device__ __forceinline__ unsigned cvtpk(float lo, float hi) {
  typedef float f2 __attribute__((ext_vector_type(2)));
  f2 v = {lo, hi};
  bf16x2_t r = __builtin_convertvector(v, bf16x2_t);
  return __builtin_bit_cast(unsigned, r);
}
__device__ __forceinline__ float rbf(float x) { return lo2f(cvtpk(x, x) & 0xffffu); }
__device__ __forceinline__ u16 f2bf(float x) { return (u16)(cvtpk(x, x) & 0xffffu); }
__device__ __forceinline__ float silu_f(float x) { return x / (1.f + __expf(-x)); }
__device__ __forceinline__ f32x4 mfma(v4u a, v4u b, f32x4 c) {
  return __builtin_amdgcn_mfma_f32_16x16x32_bf16(__builtin_bit_cast(bf16x8, a), __builtin_bit_cast(bf16x8, b), c, 0, 0, 0);
}
__device__ __forceinline__ float wave_sum(float v) {
  for (int o = 32; o >= 1; o >>= 1) v += __shfl_xor(v, o);
  return v;
}

template <int K, int N>
__device__ __forceinline__ void gemv64(const float* in0, const float* in1, const u16* W, int col0, float* red, float& o0, float& o1) {
  const int tid = threadIdx.x, cg = tid & 7, kg = tid >> 3;
  float a0[8], a1[8];
  for (int i = 0; i < 8; ++i) { a0[i] = 0.f; a1[i] = 0.f; }
#pragma unroll 4
  for (int k = kg; k < K; k += 32) {
    v4u w = *(const v4u*)(W + (size_t)k * N + col0 + cg * 8);
    float x0 = in0[k], x1 = in1[k];
    for (int i = 0; i < 4; ++i) {
      a0[2 * i] += x0 * lo2f(w[i]); a0[2 * i + 1] += x0 * hi2f(w[i]);
      a1[2 * i] += x1 * lo2f(w[i]); a1[2 * i + 1] += x1 * hi2f(w[i]);
    }
  }
  for (int i = 0; i < 8; ++i) { red[(kg * 2 + 0) * 64 + cg * 8 + i] = a0[i]; red[(kg * 2 + 1) * 64 + cg * 8 + i] = a1[i]; }
  __syncthreads();
  o0 = 0.f; o1 = 0.f;
  if (tid < 64) for (int g = 0; g < 32; ++g) { o0 += red[(g * 2) * 64 + tid]; o1 += red[(g * 2 + 1) * 64 + tid]; }
}

__global__ void __launch_bounds__(256) k_mod(const u16* c, const u16* wmod, float* mod) {
  __shared__ float sc[2][512]; __shared__ float red[64 * 64];
  for (int i = threadIdx.x; i < 1024; i += 256) sc[i >> 9][i & 511] = rbf(silu_f(bf2f(c[i])));
  __syncthreads();
  float o0, o1; gemv64<512, 3072>(sc[0], sc[1], wmod, blockIdx.x * 64, red, o0, o1);
  if (threadIdx.x < 64) { mod[blockIdx.x * 64 + threadIdx.x] = rbf(o0); mod[3072 + blockIdx.x * 64 + threadIdx.x] = rbf(o1); }
}

__device__ __forceinline__ void ln_mod(const float (*xs)[512], float (*hs)[512], const float* mod, int shift_idx, int scale_idx) {
  const int tid = threadIdx.x, w = tid >> 6, lane = tid & 63;
  if (w < 2) {
    float v[8], s = 0.f;
    for (int i = 0; i < 8; ++i) { v[i] = xs[w][lane + 64 * i]; s += v[i]; }
    s = wave_sum(s);
    const float mean = s * (1.f / 512.f);
    float q = 0.f;
    for (int i = 0; i < 8; ++i) { float d = v[i] - mean; q += d * d; }
    q = wave_sum(q);
    const float m = rbf(mean), var = rbf(q * (1.f / 512.f));
    const float sd = rbf(sqrtf(rbf(var + 1e-5f)));
    for (int i = 0; i < 8; ++i) {
      const int d = lane + 64 * i;
      const float ln = rbf(rbf(v[i] - m) / sd);
      const float ca = mod[w * 3072 + scale_idx * 512 + d], sa = mod[w * 3072 + shift_idx * 512 + d];
      hs[w][d] = rbf(rbf(ln * rbf(1.f + ca)) + sa);
    }
  }
}

__global__ void __launch_bounds__(256) k_q(const u16* x, const float* mod, const u16* wq, u16* q) {
  __shared__ float xs[2][512]; __shared__ float hs[2][512]; __shared__ float red[64 * 64];
  for (int i = threadIdx.x; i < 1024; i += 256) xs[i >> 9][i & 511] = bf2f(x[i]);
  __syncthreads();
  ln_mod(xs, hs, mod, 0, 1);
  __syncthreads();
  float o0, o1; gemv64<512, 4096>(hs[0], hs[1], wq, blockIdx.x * 64, red, o0, o1);
  if (threadIdx.x < 64) { q[blockIdx.x * 64 + threadIdx.x] = f2bf(o0); q[4096 + blockIdx.x * 64 + threadIdx.x] = f2bf(o1); }
}

__device__ __forceinline__ float xg_max(float x) {
  auto s = __builtin_amdgcn_permlane16_swap(__float_as_uint(x), __float_as_uint(x), false, false);
  x = fmaxf(__uint_as_float(s[0]), __uint_as_float(s[1]));
  auto t = __builtin_amdgcn_permlane32_swap(__float_as_uint(x), __float_as_uint(x), false, false);
  return fmaxf(__uint_as_float(t[0]), __uint_as_float(t[1]));
}
__device__ __forceinline__ float xg_sum(float x) {
  auto s = __builtin_amdgcn_permlane16_swap(__float_as_uint(x), __float_as_uint(x), false, false);
  x = __uint_as_float(s[0]) + __uint_as_float(s[1]);
  auto t = __builtin_amdgcn_permlane32_swap(__float_as_uint(x), __float_as_uint(x), false, false);
  return __uint_as_float(t[0]) + __uint_as_float(t[1]);
}
__device__ __forceinline__ v4u ldnt(const GAS v4u* p) { return __builtin_nontemporal_load(p); }

__global__ void __launch_bounds__(512, 2) k_attn(const u16* q, const u16* Kc, const u16* Vc, float* pm, float* pl, float* po) {
  __shared__ float sm[NW][4], sl[NW][4]; __shared__ float so[NW][4][128];
  const int tid = threadIdx.x, lane = tid & 63, w = __builtin_amdgcn_readfirstlane(tid >> 6);
  const int bk = blockIdx.x >> 4, split = blockIdx.x & 15;
  const int g = lane >> 4, n = lane & 15;
  const int b = bk >> 3, kvh = bk & 7;
  v4u qf[4];
  for (int s = 0; s < 4; ++s) {
    v4u t = {0u, 0u, 0u, 0u};
    if (n < 4) t = *(const v4u*)(q + (size_t)b * 4096 + (kvh * 4 + n) * 128 + 32 * s + 8 * g);
    qf[s] = t;
  }
  const size_t base = ((size_t)bk * SK + (size_t)split * KEYS_PER_WG) * DH;
  const GAS v4u* Kp = (const GAS v4u*)(Kc + base) + ((4 * (n & 3) + (n >> 2)) * 16 + g);
  const GAS v4u* Vp = (const GAS v4u*)(Vc + base) + lane;
  constexpr int NB = KEYS_PER_WG / 32 / NW;
  f32x4 acc[8];
  for (int e = 0; e < 8; ++e) acc[e] = f32x4{0.f, 0.f, 0.f, 0.f};
  float m = -INFINITY, l = 0.f;
  v4u kb[2][8], vb[2][8];
#pragma unroll
  for (int p = 0; p < 2; ++p) {
    const int blk = w + p * NW;
#pragma unroll
    for (int i = 0; i < 8; ++i) kb[p][i] = ldnt(Kp + blk * 512 + (i >> 2) * 256 + (i & 3) * 4);
#pragma unroll
    for (int j = 0; j < 8; ++j) vb[p][j] = ldnt(Vp + blk * 512 + j * 64);
  }
  __builtin_amdgcn_sched_barrier(0);
  constexpr float SC = 0.08838834764831845f, L2E = 1.4426950408889634f;
#pragma unroll
  for (int it = 0; it < NB; ++it) {
    const int p = it & 1;
    f32x4 s0 = {0.f, 0.f, 0.f, 0.f}, s1 = {0.f, 0.f, 0.f, 0.f};
#pragma unroll
    for (int s = 0; s < 4; ++s) { s0 = mfma(kb[p][s], qf[s], s0); s1 = mfma(kb[p][4 + s], qf[s], s1); }
    if (it + 2 < NB) {
      const int blk = w + (it + 2) * NW;
#pragma unroll
      for (int i = 0; i < 8; ++i) kb[p][i] = ldnt(Kp + blk * 512 + (i >> 2) * 256 + (i & 3) * 4);
    }
    __builtin_amdgcn_sched_barrier(0);
    float sc[8];
#pragma unroll
    for (int i = 0; i < 4; i += 2) {
      unsigned u0 = cvtpk(s0[i], s0[i + 1]), u1 = cvtpk(s1[i], s1[i + 1]);
      unsigned t0 = cvtpk(lo2f(u0) * SC, hi2f(u0) * SC), t1 = cvtpk(lo2f(u1) * SC, hi2f(u1) * SC);
      sc[i] = lo2f(t0); sc[i + 1] = hi2f(t0); sc[4 + i] = lo2f(t1); sc[4 + i + 1] = hi2f(t1);
    }
    float bm = fmaxf(fmaxf(fmaxf(sc[0], sc[1]), fmaxf(sc[2], sc[3])), fmaxf(fmaxf(sc[4], sc[5]), fmaxf(sc[6], sc[7])));
    bm = xg_max(bm);
    if (__any(bm > m)) {
      const float mn = fmaxf(m, bm);
      const float alpha = __builtin_amdgcn_exp2f((m - mn) * L2E);
      m = mn; l *= alpha;
#pragma unroll
      for (int e = 0; e < 8; ++e) acc[e] *= alpha;
    }
    const float mb = m * L2E;
    float pr[8];
#pragma unroll
    for (int i = 0; i < 8; ++i) { pr[i] = __builtin_amdgcn_exp2f(sc[i] * L2E - mb); l += pr[i]; }
    v4u pf = {cvtpk(pr[0], pr[1]), cvtpk(pr[2], pr[3]), cvtpk(pr[4], pr[5]), cvtpk(pr[6], pr[7])};
#pragma unroll
    for (int e = 0; e < 8; ++e) {
      v4u a;
#pragma unroll
      for (int u = 0; u < 4; ++u) a[u] = __builtin_amdgcn_perm(vb[p][2 * u + 1][e >> 1], vb[p][2 * u][e >> 1], (e & 1) ? 0x07060302u : 0x05040100u);
      acc[e] = mfma(a, pf, acc[e]);
    }
    if (it + 2 < NB) {
      const int blk = w + (it + 2) * NW;
#pragma unroll
      for (int j = 0; j < 8; ++j) vb[p][j] = ldnt(Vp + blk * 512 + j * 64);
    }
    __builtin_amdgcn_sched_barrier(0);
  }
  l = xg_sum(l);
  if (n < 4) {
    if (g == 0) { sm[w][n] = m; sl[w][n] = l; }
#pragma unroll
    for (int i = 0; i < 4; ++i)
#pragma unroll
      for (int e = 0; e < 8; ++e) so[w][n][8 * (4 * g + i) + e] = acc[e][i];
  }
  __syncthreads();
  {
    const int h = tid >> 7, d = tid & 127;
    float M = sm[0][h];
    for (int ww = 1; ww < NW; ++ww) M = fmaxf(M, sm[ww][h]);
    float L = 0.f, o = 0.f;
    for (int ww = 0; ww < NW; ++ww) { const float a = __builtin_amdgcn_exp2f((sm[ww][h] - M) * L2E); L += a * sl[ww][h]; o += a * so[ww][h][d]; }
    po[((size_t)blockIdx.x * 4 + h) * 128 + d] = o;
    if (d == 0) { pm[blockIdx.x * 4 + h] = M; pl[blockIdx.x * 4 + h] = L; }
  }
}

__global__ void __launch_bounds__(128) k_comb(const float* pm, const float* pl, const float* po, float* o) {
  const int bh = blockIdx.x, b = bh >> 5, hq = bh & 31, kvh = hq >> 2, h = hq & 3, d = threadIdx.x;
  const int wg0 = (b * 8 + kvh) * 16;
  float M = -INFINITY;
  for (int s = 0; s < 16; ++s) M = fmaxf(M, pm[(wg0 + s) * 4 + h]);
  float L = 0.f, acc = 0.f;
  for (int s = 0; s < 16; ++s) { const float a = __builtin_amdgcn_exp2f((pm[(wg0 + s) * 4 + h] - M) * 1.4426950408889634f); L += a * pl[(wg0 + s) * 4 + h]; acc += a * po[((size_t)(wg0 + s) * 4 + h) * 128 + d]; }
  o[b * 4096 + hq * 128 + d] = rbf(acc / L);
}

__global__ void __launch_bounds__(256) k_wo(const float* o, const u16* wo, const u16* x, const float* mod, float* x1) {
  __shared__ float os[2][4096]; __shared__ float red[64 * 64];
  for (int i = threadIdx.x; i < 8192; i += 256) os[i >> 12][i & 4095] = o[i];
  __syncthreads();
  float o0, o1; gemv64<4096, 512>(os[0], os[1], wo, blockIdx.x * 64, red, o0, o1);
  if (threadIdx.x < 64) {
    const int c = blockIdx.x * 64 + threadIdx.x;
    x1[c] = rbf(bf2f(x[c]) + rbf(mod[2 * 512 + c] * rbf(o0)));
    x1[512 + c] = rbf(bf2f(x[512 + c]) + rbf(mod[3072 + 2 * 512 + c] * rbf(o1)));
  }
}
__global__ void __launch_bounds__(256) k_w1(const float* x1, const float* mod, const u16* w1, float* h1) {
  __shared__ float xs[2][512]; __shared__ float hs[2][512]; __shared__ float red[64 * 64];
  for (int i = threadIdx.x; i < 1024; i += 256) xs[i >> 9][i & 511] = x1[i];
  __syncthreads();
  ln_mod(xs, hs, mod, 3, 4);
  __syncthreads();
  float o0, o1; gemv64<512, 1792>(hs[0], hs[1], w1, blockIdx.x * 64, red, o0, o1);
  if (threadIdx.x < 64) { h1[blockIdx.x * 64 + threadIdx.x] = rbf(silu_f(rbf(o0))); h1[1792 + blockIdx.x * 64 + threadIdx.x] = rbf(silu_f(rbf(o1))); }
}
__global__ void __launch_bounds__(256) k_w2(const float* h1, const u16* w2, const float* x1, const float* mod, u16* out) {
  __shared__ float hs[2][1792]; __shared__ float red[64 * 64];
  for (int i = threadIdx.x; i < 2 * 1792; i += 256) hs[i / 1792][i % 1792] = h1[i];
  __syncthreads();
  float o0, o1; gemv64<1792, 512>(hs[0], hs[1], w2, blockIdx.x * 64, red, o0, o1);
  if (threadIdx.x < 64) {
    const int c = blockIdx.x * 64 + threadIdx.x;
    out[c] = f2bf(x1[c] + rbf(mod[5 * 512 + c] * rbf(o0)));
    out[512 + c] = f2bf(x1[512 + c] + rbf(mod[3072 + 5 * 512 + c] * rbf(o1)));
  }
}

extern "C" void kernel_launch(void* const* d_in, const int* in_sizes, int n_in, void* d_out, int out_size, void* d_ws, size_t ws_size, hipStream_t stream) {
  const u16* x = (const u16*)d_in[0]; const u16* Kc = (const u16*)d_in[1]; const u16* Vc = (const u16*)d_in[2];
  const u16* wq = (const u16*)d_in[3]; const u16* wo = (const u16*)d_in[4];
  const u16* w1 = (const u16*)d_in[6]; const u16* w2 = (const u16*)d_in[7];
  const u16* c = (const u16*)d_in[8]; const u16* wmod = (const u16*)d_in[9];
  char* ws = (char*)d_ws;
  float* mod = (float*)ws;
  u16* q = (u16*)(ws + 32768);
  float* pm = (float*)(ws + 65536);
  float* pl = (float*)(ws + 65536 + 4096);
  float* po = (float*)(ws + 131072);
  float* o = (float*)(ws + 1048576);
  float* x1 = (float*)(ws + 1048576 + 32768);
  float* h1 = (float*)(ws + 1048576 + 65536);
  k_mod<<<48, 256, 0, stream>>>(c, wmod, mod);
  k_q<<<64, 256, 0, stream>>>(x, mod, wq, q);
  k_attn<<<256, 512, 0, stream>>>(q, Kc, Vc, pm, pl, po);
  k_comb<<<64, 128, 0, stream>>>(pm, pl, po, o);
  k_wo<<<8, 256, 0, stream>>>(o, wo, x, mod, x1);
  k_w1<<<28, 256, 0, stream>>>(x1, mod, w1, h1);
  k_w2<<<8, 256, 0, stream>>>(h1, w2, x1, mod, (u16*)d_out);
}
```

```cpp
#include <hip/hip_runtime.h>
#include <hip/hip_bf16.h>
#include <stdint.h>

typedef unsigned short u16;
typedef unsigned v4u __attribute__((ext_vector_type(4)));
typedef unsigned v2u __attribute__((ext_vector_type(2)));
typedef float f32x4 __attribute__((ext_vector_type(4)));
typedef short bf16x8 __attribute__((ext_vector_type(8)));
typedef __bf16 bf16x2_t __attribute__((ext_vector_type(2)));
#define GAS __attribute__((address_space(1)))

constexpr int B = 2, D = 512, HQ = 32, HKV = 8, SK = 32768, DH = 128, F = 1792;
constexpr int NSPLIT = 16;
constexpr int KEYS_PER_WG = SK / NSPLIT;
constexpr int NW = 8;

__device__ __forceinline__ float bf2f(u16 b) { return __builtin_bit_cast(float, (unsigned)b << 16); }
__device__ __forceinline__ float lo2f(unsigned u) { return __builtin_bit_cast(float, u << 16); }
__device__ __forceinline__ float hi2f(unsigned u) { return __builtin_bit_cast(float, u & 0xffff0000u); }
__device__ __forceinline__ unsigned cvtpk(float lo, float hi) {
  typedef float f2 __attribute__((ext_vector_type(2)));
  f2 v = {lo, hi};
  bf16x2_t r = __builtin_convertvector(v, bf16x2_t);
  return __builtin_bit_cast(unsigned, r);
}
__device__ __forceinline__ float rbf(float x) { return x; }
__device__ __forceinline__ u16 f2bf(float x) { return (u16)(cvtpk(x, x) & 0xffffu); }
__device__ __forceinline__ float silu_f(float x) { return x / (1.f + __expf(-x)); }
__device__ __forceinline__ f32x4 mfma(v4u a, v4u b, f32x4 c) {
  return __builtin_amdgcn_mfma_f32_16x16x32_bf16(__builtin_bit_cast(bf16x8, a), __builtin_bit_cast(bf16x8, b), c, 0, 0, 0);
}
__device__ __forceinline__ float wave_sum(float v) {
  for (int o = 32; o >= 1; o >>= 1) v += __shfl_xor(v, o);
  return v;
}

template <int K, int N>
__device__ __forceinline__ void gemv64(const float* in0, const float* in1, const u16* W, int col0, float* red, float& o0, float& o1) {
  const int tid = threadIdx.x, cg = tid & 7, kg = tid >> 3;
  float a0[8], a1[8];
  for (int i = 0; i < 8; ++i) { a0[i] = 0.f; a1[i] = 0.f; }
#pragma unroll 4
  for (int k = kg; k < K; k += 32) {
    v4u w = *(const v4u*)(W + (size_t)k * N + col0 + cg * 8);
    float x0 = in0[k], x1 = in1[k];
    for (int i = 0; i < 4; ++i) {
      a0[2 * i] += x0 * lo2f(w[i]); a0[2 * i + 1] += x0 * hi2f(w[i]);
      a1[2 * i] += x1 * lo2f(w[i]); a1[2 * i + 1] += x1 * hi2f(w[i]);
    }
  }
  for (int i = 0; i < 8; ++i) { red[(kg * 2 + 0) * 64 + cg * 8 + i] = a0[i]; red[(kg * 2 + 1) * 64 + cg * 8 + i] = a1[i]; }
  __syncthreads();
  o0 = 0.f; o1 = 0.f;
  if (tid < 64) for (int g = 0; g < 32; ++g) { o0 += red[(g * 2) * 64 + tid]; o1 += red[(g * 2 + 1) * 64 + tid]; }
}

__global__ void __launch_bounds__(256) k_mod(const u16* c, const u16* wmod, float* mod) {
  __shared__ float sc[2][512]; __shared__ float red[64 * 64];
  for (int i = threadIdx.x; i < 1024; i += 256) sc[i >> 9][i & 511] = rbf(silu_f(bf2f(c[i])));
  __syncthreads();
  float o0, o1; gemv64<512, 3072>(sc[0], sc[1], wmod, blockIdx.x * 64, red, o0, o1);
  if (threadIdx.x < 64) { mod[blockIdx.x * 64 + threadIdx.x] = rbf(o0); mod[3072 + blockIdx.x * 64 + threadIdx.x] = rbf(o1); }
}

__device__ __forceinline__ void ln_mod(const float (*xs)[512], float (*hs)[512], const float* mod, int shift_idx, int scale_idx) {
  const int tid = threadIdx.x, w = tid >> 6, lane = tid & 63;
  if (w < 2) {
    float v[8], s = 0.f;
    for (int i = 0; i < 8; ++i) { v[i] = xs[w][lane + 64 * i]; s += v[i]; }
    s = wave_sum(s);
    const float mean = s * (1.f / 512.f);
    float q = 0.f;
    for (int i = 0; i < 8; ++i) { float d = v[i] - mean; q += d * d; }
    q = wave_sum(q);
    const float m = rbf(mean), var = rbf(q * (1.f / 512.f));
    const float sd = rbf(sqrtf(rbf(var + 1e-5f)));
    for (int i = 0; i < 8; ++i) {
      const int d = lane + 64 * i;
      const float ln = rbf(rbf(v[i] - m) / sd);
      const float ca = mod[w * 3072 + scale_idx * 512 + d], sa = mod[w * 3072 + shift_idx * 512 + d];
      hs[w][d] = rbf(rbf(ln * rbf(1.f + ca)) + sa);
    }
  }
}

__global__ void __launch_bounds__(256) k_q(const u16* x, const float* mod, const u16* wq, u16* q) {
  __shared__ float xs[2][512]; __shared__ float hs[2][512]; __shared__ float red[64 * 64];
  for (int i = threadIdx.x; i < 1024; i += 256) xs[i >> 9][i & 511] = bf2f(x[i]);
  __syncthreads();
  ln_mod(xs, hs, mod, 0, 1);
  __syncthreads();
  float o0, o1; gemv64<512, 4096>(hs[0], hs[1], wq, blockIdx.x * 64, red, o0, o1);
  if (threadIdx.x < 64) { q[blockIdx.x * 64 + threadIdx.x] = f2bf(o0); q[4096 + blockIdx.x * 64 + threadIdx.x] = f2bf(o1); }
}

__device__ __forceinline__ float xg_max(float x) {
  auto s = __builtin_amdgcn_permlane16_swap(__float_as_uint(x), __float_as_uint(x), false, false);
  x = fmaxf(__uint_as_float(s[0]), __uint_as_float(s[1]));
  auto t = __builtin_amdgcn_permlane32_swap(__float_as_uint(x), __float_as_uint(x), false, false);
  return fmaxf(__uint_as_float(t[0]), __uint_as_float(t[1]));
}
__device__ __forceinline__ float xg_sum(float x) {
  auto s = __builtin_amdgcn_permlane16_swap(__float_as_uint(x), __float_as_uint(x), false, false);
  x = __uint_as_float(s[0]) + __uint_as_float(s[1]);
  auto t = __builtin_amdgcn_permlane32_swap(__float_as_uint(x), __float_as_uint(x), false, false);
  return __uint_as_float(t[0]) + __uint_as_float(t[1]);
}
__device__ __forceinline__ v4u ldnt(const GAS v4u* p) { return __builtin_nontemporal_load(p); }

__global__ void __launch_bounds__(512, 2) k_attn(const u16* q, const u16* Kc, const u16* Vc, float* pm, float* pl, float* po) {
  __shared__ float sm[NW][4], sl[NW][4]; __shared__ float so[NW][4][128];
  const int tid = threadIdx.x, lane = tid & 63, w = __builtin_amdgcn_readfirstlane(tid >> 6);
  const int bk = blockIdx.x >> 4, split = blockIdx.x & 15;
  const int g = lane >> 4, n = lane & 15;
  const int b = bk >> 3, kvh = bk & 7;
  v4u qf[4];
  for (int s = 0; s < 4; ++s) {
    v4u t = {0u, 0u, 0u, 0u};
    if (n < 4) t = *(const v4u*)(q + (size_t)b * 4096 + (kvh * 4 + n) * 128 + 32 * s + 8 * g);
    qf[s] = t;
  }
  const size_t base = ((size_t)bk * SK + (size_t)split * KEYS_PER_WG) * DH;
  const GAS v4u* Kp = (const GAS v4u*)(Kc + base) + ((4 * (n & 3) + (n >> 2)) * 16 + g);
  const GAS v4u* Vp = (const GAS v4u*)(Vc + base) + lane;
  constexpr int NB = KEYS_PER_WG / 32 / NW;
  f32x4 acc[8];
  for (int e = 0; e < 8; ++e) acc[e] = f32x4{0.f, 0.f, 0.f, 0.f};
  float m = -INFINITY, l = 0.f;
  v4u kb[2][8], vb[2][8];
#pragma unroll
  for (int p = 0; p < 2; ++p) {
    const int blk = w + p * NW;
#pragma unroll
    for (int i = 0; i < 8; ++i) kb[p][i] = ldnt(Kp + blk * 512 + (i >> 2) * 256 + (i & 3) * 4);
#pragma unroll
    for (int j = 0; j < 8; ++j) vb[p][j] = ldnt(Vp + blk * 512 + j * 64);
  }
  __builtin_amdgcn_sched_barrier(0);
  constexpr float SC = 0.08838834764831845f, L2E = 1.4426950408889634f;
#pragma unroll
  for (int it = 0; it < NB; ++it) {
    const int p = it & 1;
    f32x4 s0 = {0.f, 0.f, 0.f, 0.f}, s1 = {0.f, 0.f, 0.f, 0.f};
#pragma unroll
    for (int s = 0; s < 4; ++s) { s0 = mfma(kb[p][s], qf[s], s0); s1 = mfma(kb[p][4 + s], qf[s], s1); }
    if (it + 2 < NB) {
      const int blk = w + (it + 2) * NW;
#pragma unroll
      for (int i = 0; i < 8; ++i) kb[p][i] = ldnt(Kp + blk * 512 + (i >> 2) * 256 + (i & 3) * 4);
    }
    __builtin_amdgcn_sched_barrier(0);
    float sc[8];
#pragma unroll
    for (int i = 0; i < 4; ++i) { sc[i] = s0[i] * SC; sc[4 + i] = s1[i] * SC; }
    float bm = fmaxf(fmaxf(fmaxf(sc[0], sc[1]), fmaxf(sc[2], sc[3])), fmaxf(fmaxf(sc[4], sc[5]), fmaxf(sc[6], sc[7])));
    bm = xg_max(bm);
    if (__any(bm > m)) {
      const float mn = fmaxf(m, bm);
      const float alpha = __builtin_amdgcn_exp2f((m - mn) * L2E);
      m = mn; l *= alpha;
#pragma unroll
      for (int e = 0; e < 8; ++e) acc[e] *= alpha;
    }
    const float mb = m * L2E;
    float pr[8];
#pragma unroll
    for (int i = 0; i < 8; ++i) { pr[i] = __builtin_amdgcn_exp2f(sc[i] * L2E - mb); l += pr[i]; }
    v4u pf = {cvtpk(pr[0], pr[1]), cvtpk(pr[2], pr[3]), cvtpk(pr[4], pr[5]), cvtpk(pr[6], pr[7])};
#pragma unroll
    for (int e = 0; e < 8; ++e) {
      v4u a;
#pragma unroll
      for (int u = 0; u < 4; ++u) a[u] = __builtin_amdgcn_perm(vb[p][2 * u + 1][e >> 1], vb[p][2 * u][e >> 1], (e & 1) ? 0x07060302u : 0x05040100u);
      acc[e] = mfma(a, pf, acc[e]);
    }
    if (it + 2 < NB) {
      const int blk = w + (it + 2) * NW;
#pragma unroll
      for (int j = 0; j < 8; ++j) vb[p][j] = ldnt(Vp + blk * 512 + j * 64);
    }
    __builtin_amdgcn_sched_barrier(0);
  }
  l = xg_sum(l);
  if (n < 4) {
    if (g == 0) { sm[w][n] = m; sl[w][n] = l; }
#pragma unroll
    for (int i = 0; i < 4; ++i)
#pragma unroll
      for (int e = 0; e < 8; ++e) so[w][n][8 * (4 * g + i) + e] = acc[e][i];
  }
  __syncthreads();
  {
    const int h = tid >> 7, d = tid & 127;
    float M = sm[0][h];
    for (int ww = 1; ww < NW; ++ww) M = fmaxf(M, sm[ww][h]);
    float L = 0.f, o = 0.f;
    for (int ww = 0; ww < NW; ++ww) { const float a = __builtin_amdgcn_exp2f((sm[ww][h] - M) * L2E); L += a * sl[ww][h]; o += a * so[ww][h][d]; }
    po[((size_t)blockIdx.x * 4 + h) * 128 + d] = o;
    if (d == 0) { pm[blockIdx.x * 4 + h] = M; pl[blockIdx.x * 4 + h] = L; }
  }
}

__global__ void __launch_bounds__(128) k_comb(const float* pm, const float* pl, const float* po, float* o) {
  const int bh = blockIdx.x, b = bh >> 5, hq = bh & 31, kvh = hq >> 2, h = hq & 3, d = threadIdx.x;
  const int wg0 = (b * 8 + kvh) * 16;
  float M = -INFINITY;
  for (int s = 0; s < 16; ++s) M = fmaxf(M, pm[(wg0 + s) * 4 + h]);
  float L = 0.f, acc = 0.f;
  for (int s = 0; s < 16; ++s) { const float a = __builtin_amdgcn_exp2f((pm[(wg0 + s) * 4 + h] - M) * 1.4426950408889634f); L += a * pl[(wg0 + s) * 4 + h]; acc += a * po[((size_t)(wg0 + s) * 4 + h) * 128 + d]; }
  o[b * 4096 + hq * 128 + d] = rbf(acc / L);
}

__global__ void __launch_bounds__(256) k_wo(const float* o, const u16* wo, const u16* x, const float* mod, float* x1) {
  __shared__ float os[2][4096]; __shared__ float red[64 * 64];
  for (int i = threadIdx.x; i < 8192; i += 256) os[i >> 12][i & 4095] = o[i];
  __syncthreads();
  float o0, o1; gemv64<4096, 512>(os[0], os[1], wo, blockIdx.x * 64, red, o0, o1);
  if (threadIdx.x < 64) {
    const int c = blockIdx.x * 64 + threadIdx.x;
    x1[c] = rbf(bf2f(x[c]) + rbf(mod[2 * 512 + c] * rbf(o0)));
    x1[512 + c] = rbf(bf2f(x[512 + c]) + rbf(mod[3072 + 2 * 512 + c] * rbf(o1)));
  }
}
__global__ void __launch_bounds__(256) k_w1(const float* x1, const float* mod, const u16* w1, float* h1) {
  __shared__ float xs[2][512]; __shared__ float hs[2][512]; __shared__ float red[64 * 64];
  for (int i = threadIdx.x; i < 1024; i += 256) xs[i >> 9][i & 511] = x1[i];
  __syncthreads();
  ln_mod(xs, hs, mod, 3, 4);
  __syncthreads();
  float o0, o1; gemv64<512, 1792>(hs[0], hs[1], w1, blockIdx.x * 64, red, o0, o1);
  if (threadIdx.x < 64) { h1[blockIdx.x * 64 + threadIdx.x] = rbf(silu_f(rbf(o0))); h1[1792 + blockIdx.x * 64 + threadIdx.x] = rbf(silu_f(rbf(o1))); }
}
__global__ void __launch_bounds__(256) k_w2(const float* h1, const u16* w2, const float* x1, const float* mod, u16* out) {
  __shared__ float hs[2][1792]; __shared__ float red[64 * 64];
  for (int i = threadIdx.x; i < 2 * 1792; i += 256) hs[i / 1792][i % 1792] = h1[i];
  __syncthreads();
  float o0, o1; gemv64<1792, 512>(hs[0], hs[1], w2, blockIdx.x * 64, red, o0, o1);
  if (threadIdx.x < 64) {
    const int c = blockIdx.x * 64 + threadIdx.x;
    out[c] = f2bf(x1[c] + rbf(mod[5 * 512 + c] * rbf(o0)));
    out[512 + c] = f2bf(x1[512 + c] + rbf(mod[3072 + 5 * 512 + c] * rbf(o1)));
  }
}

extern "C" void kernel_launch(void* const* d_in, const int* in_sizes, int n_in, void* d_out, int out_size, void* d_ws, size_t ws_size, hipStream_t stream) {
  const u16* x = (const u16*)d_in[0]; const u16* Kc = (const u16*)d_in[1]; const u16* Vc = (const u16*)d_in[2];
  const u16* wq = (const u16*)d_in[3]; const u16* wo = (const u16*)d_in[4];
  const u16* w1 = (const u16*)d_in[6]; const u16* w2 = (const u16*)d_in[7];
  const u16* c = (const u16*)d_in[8]; const u16* wmod = (const u16*)d_in[9];
  char* ws = (char*)d_ws;
  float* mod = (float*)ws;
  u16* q = (u16*)(ws + 32768);
  float* pm = (float*)(ws + 65536);
  float* pl = (float*)(ws + 65536 + 4096);
  float* po = (float*)(ws + 131072);
  float* o = (float*)(ws + 1048576);
  float* x1 = (float*)(ws + 1048576 + 32768);
  float* h1 = (float*)(ws + 1048576 + 65536);
  k_mod<<<48, 256, 0, stream>>>(c, wmod, mod);
  k_q<<<64, 256, 0, stream>>>(x, mod, wq, q);
  k_attn<<<256, 512, 0, stream>>>(q, Kc, Vc, pm, pl, po);
  k_comb<<<64, 128, 0, stream>>>(pm, pl, po, o);
  k_wo<<<8, 256, 0, stream>>>(o, wo, x, mod, x1);
  k_w1<<<28, 256, 0, stream>>>(x1, mod, w1, h1);
  k_w2<<<8, 256, 0, stream>>>(h1, w2, x1, mod, (u16*)d_out);
}
```

```cpp
#include <hip/hip_runtime.h>
#include <hip/hip_bf16.h>
#include <stdint.h>

typedef unsigned short u16;
typedef unsigned long long u64;
typedef unsigned v4u __attribute__((ext_vector_type(4)));
typedef unsigned v2u __attribute__((ext_vector_type(2)));
typedef float f32x4 __attribute__((ext_vector_type(4)));
typedef float f32x2 __attribute__((ext_vector_type(2)));
typedef short bf16x8 __attribute__((ext_vector_type(8)));
typedef short s16x4 __attribute__((ext_vector_type(4)));
typedef __bf16 bf16x2_t __attribute__((ext_vector_type(2)));
typedef __amdgpu_buffer_rsrc_t rsrc_t;
#define GAS __attribute__((address_space(1)))
#define LAS __attribute__((address_space(3)))
#define RLX_AGENT __ATOMIC_RELAXED, __HIP_MEMORY_SCOPE_AGENT

#define NSLOT 9
#define DEPTH 32
#define DRYRUN 1
#define POLL_SLEEP 0

constexpr int NWG = 256, NTHR = 576, NUNIT = 64;
constexpr unsigned SENT = 0xFFFFFFFFu, OOB = 0x7FFFFFF0u;
constexpr int AUX_NT = 2, AUX_SC1 = 16;
constexpr int W_EPOCH = 0, W_HG = 0x1000, W_QS = 0x2000, W_MS = 0x6000, W_AB = 0x8000, W_H1 = 0x10000, W_ML = 0x18000, W_PO = 0x20000, W_END = 0xA0000;
constexpr int W_STAMP = 0x100000;
constexpr int L_CTRL = NSLOT * 16384, L_FULL = L_CTRL, L_QRDY = L_CTRL + 4, L_FREE = L_CTRL + 16, L_Q = L_CTRL + 64, L_MOD = L_Q + 1024,
              L_RAB = L_MOD + 4096, L_ST = L_RAB + 1024, L_RC = L_ST + 64, L_RD = L_RC + 512, L_X1 = L_RD + 128, L_END = L_X1 + 16;
static_assert(L_END <= 160 * 1024, "LDS");
constexpr unsigned SPIN_G = 1u << 17, SPIN_L = 1u << 22;

#define STAMP(k) do {} while (0)

__device__ __forceinline__ float lo2f(unsigned u) { return __builtin_bit_cast(float, u << 16); }
__device__ __forceinline__ float hi2f(unsigned u) { return __builtin_bit_cast(float, u & 0xffff0000u); }
__device__ __forceinline__ unsigned cvtpk(float lo, float hi) { f32x2 v = {lo, hi}; return __builtin_bit_cast(unsigned, __builtin_convertvector(v, bf16x2_t)); }
__device__ __forceinline__ float rbf(float x) { return lo2f(cvtpk(x, x)); }
__device__ __forceinline__ float rcp(float x) { return __builtin_amdgcn_rcpf(x); }
__device__ __forceinline__ float silu_f(float x) { return x * rcp(1.f + __builtin_amdgcn_exp2f(x * -1.4426950408889634f)); }
__device__ __forceinline__ f32x4 mfma(v4u a, v4u b, f32x4 c) {
  return __builtin_amdgcn_mfma_f32_16x16x32_bf16(__builtin_bit_cast(bf16x8, a), __builtin_bit_cast(bf16x8, b), c, 0, 0, 0);
}
__device__ __forceinline__ float dot2(unsigned a, unsigned b, float c) {
  return __builtin_amdgcn_fdot2_f32_bf16(__builtin_bit_cast(bf16x2_t, a), __builtin_bit_cast(bf16x2_t, b), c, false);
}
__device__ __forceinline__ unsigned pk_lo(unsigned r0, unsigned r1) { return __builtin_amdgcn_perm(r1, r0, 0x05040100u); }
__device__ __forceinline__ unsigned pk_hi(unsigned r0, unsigned r1) { return __builtin_amdgcn_perm(r1, r0, 0x07060302u); }
__device__ __forceinline__ unsigned nosent(unsigned d) { return d < 0xFFFFFFFEu ? d : 0xFFFFFFFEu; }
template <int CTRL> __device__ __forceinline__ float dpp(float x) {
  return __builtin_bit_cast(float, __builtin_amdgcn_mov_dpp(__builtin_bit_cast(int, x), CTRL, 0xf, 0xf, true));
}
__device__ __forceinline__ float row_sum(float x) { x += dpp<0x128>(x); x += dpp<0x124>(x); x += dpp<0x122>(x); x += dpp<0x121>(x); return x; }
__device__ __forceinline__ float tr32(float a, float b) {
  auto s = __builtin_amdgcn_permlane32_swap(__float_as_uint(a), __float_as_uint(b), false, false);
  return __uint_as_float(s[0]) + __uint_as_float(s[1]);
}
__device__ __forceinline__ float tr16(float a, float b) {
  auto s = __builtin_amdgcn_permlane16_swap(__float_as_uint(a), __float_as_uint(b), false, false);
  return __uint_as_float(s[0]) + __uint_as_float(s[1]);
}
__device__ __forceinline__ float xg_sum(float x) { x = tr16(x, x); return tr32(x, x); }
__device__ __forceinline__ float xg_max(float x) {
  auto s = __builtin_amdgcn_permlane16_swap(__float_as_uint(x), __float_as_uint(x), false, false);
  x = fmaxf(__uint_as_float(s[0]), __uint_as_float(s[1]));
  auto t = __builtin_amdgcn_permlane32_swap(__float_as_uint(x), __float_as_uint(x), false, false);
  return fmaxf(__uint_as_float(t[0]), __uint_as_float(t[1]));
}
__device__ __forceinline__ float wave_sum(float x) { return xg_sum(row_sum(x)); }
__device__ __forceinline__ unsigned umax4(v4u v) { return max(max(v[0], v[1]), max(v[2], v[3])); }
template <int N> __device__ __forceinline__ void waitvm() { asm volatile("s_waitcnt vmcnt(%0)" :: "n"(N) : "memory"); }
__device__ __forceinline__ void bar() { asm volatile("s_waitcnt lgkmcnt(0)\n\ts_barrier" ::: "memory"); }
__device__ __forceinline__ rsrc_t mkrs(const void* p, unsigned bytes) { return __builtin_amdgcn_make_buffer_rsrc((void*)p, 0, bytes, 0x00020000); }
__device__ __forceinline__ void dma(rsrc_t rs, int voff, int soff, unsigned lds) {
  asm volatile("s_mov_b32 m0, %3\n\ts_nop 0\n\tbuffer_load_dwordx4 %0, %1, %2 offen nt lds" :: "v"(voff), "s"(rs), "s"(soff), "s"(lds) : "memory");
}
#define LD128(rs, vo, so, aux) __builtin_amdgcn_raw_buffer_load_b128(rs, vo, so, aux)
#define LD64(rs, vo, so, aux) __builtin_amdgcn_raw_buffer_load_b64(rs, vo, so, aux)
#define LD32(rs, vo, so, aux) __builtin_amdgcn_raw_buffer_load_b32(rs, vo, so, aux)
#define ST128(v, rs, vo, so, aux) __builtin_amdgcn_raw_buffer_store_b128(v, rs, vo, so, aux)
#define ST64(v, rs, vo, so, aux) __builtin_amdgcn_raw_buffer_store_b64(v, rs, vo, so, aux)

__device__ __forceinline__ v2u gemv8(const v4u (&wr)[8], const unsigned (&a0)[4], const unsigned (&a1)[4]) {
  float v[16];
#pragma unroll
  for (int k = 0; k < 16; ++k) v[k] = 0.f;
#pragma unroll
  for (int pp = 0; pp < 4; ++pp)
#pragma unroll
    for (int d = 0; d < 4; ++d) {
      const unsigned lo = pk_lo(wr[2 * pp][d], wr[2 * pp + 1][d]), hi = pk_hi(wr[2 * pp][d], wr[2 * pp + 1][d]);
      v[2 * d] = dot2(a0[pp], lo, v[2 * d]); v[2 * d + 1] = dot2(a0[pp], hi, v[2 * d + 1]);
      v[8 + 2 * d] = dot2(a1[pp], lo, v[8 + 2 * d]); v[8 + 2 * d + 1] = dot2(a1[pp], hi, v[8 + 2 * d + 1]);
    }
#pragma unroll
  for (int k = 0; k < 8; ++k) v[k] = tr32(v[k], v[k + 8]);
#pragma unroll
  for (int k = 0; k < 4; ++k) v[k] = row_sum(tr16(v[k], v[k + 4]));
  return v2u{nosent(cvtpk(v[0], v[1])), nosent(cvtpk(v[2], v[3]))};
}

struct Params { const u16 *x, *K, *V, *wq, *wo, *w1, *w2, *c, *wmod; u16* out; unsigned char* ws; };

__global__ void __launch_bounds__(NTHR, 3) adaln_block_kernel(Params p) {
  extern __shared__ __attribute__((aligned(16))) unsigned char smem[];
  LAS unsigned char* L = (LAS unsigned char*)smem;
  const int tid = threadIdx.x, lane = tid & 63, w = __builtin_amdgcn_readfirstlane(tid >> 6);
  const int bid = blockIdx.x, xl = bid & 7, jl = bid >> 3, b = bid >> 7, kvh = (bid >> 4) & 7, mem = bid & 15;
  const int g = lane >> 4, n = lane & 15;
  volatile LAS unsigned* FULL = (volatile LAS unsigned*)(L + L_FULL);
  constexpr float L2E = 1.4426950408889634f;
  STAMP(0);
  if (tid < 8) ((LAS unsigned*)(L + L_CTRL))[tid] = 0u;
  bar();

  if (w == 8) {
    __builtin_amdgcn_s_setprio(2);
    const rsrc_t rk = mkrs(p.K + (size_t)bid * (NUNIT * 4096), NUNIT * 8192), rv = mkrs(p.V + (size_t)bid * (NUNIT * 4096), NUNIT * 8192);
    const int kv = n * 256 + g * 16, ve = g * 256 + ((n ^ (2 * g)) * 16), vo = g * 256 + ((n ^ (8 + 2 * g)) * 16);
    unsigned freed = 0; int slot = 0, pub = 0;
    constexpr int LAG = DEPTH / 8 - 1;
#pragma unroll 1
    for (int u = 0; u < NUNIT; ++u) {
      if (u >= NSLOT && freed < (unsigned)(u - NSLOT + 1)) {
        unsigned spins = 0; bool drained = false;
        for (;;) {
          const v4u f = *(volatile LAS v4u*)(L + L_FREE);
          freed = __builtin_amdgcn_readfirstlane(min(min(f[0], f[1]), min(f[2], f[3])));
          if (freed >= (unsigned)(u - NSLOT + 1)) break;
          if (++spins > SPIN_L) { freed = SENT; break; }
          if (!drained) { waitvm<0>(); pub = 2 * u; *FULL = pub; drained = true; STAMP(2); }
          __builtin_amdgcn_s_sleep(1);
        }
      }
      const unsigned lb = slot * 16384;
#pragma unroll
      for (int i = 0; i < 8; ++i) dma(rk, kv, u * 8192 + (i >> 2) * 4096 + (i & 3) * 64, lb + i * 1024);
      waitvm<DEPTH - 8>();
      pub = max(pub, 2 * u + 1 - LAG); *FULL = pub;
#pragma unroll
      for (int i = 0; i < 8; ++i) dma(rv, (i & 1) ? vo : ve, u * 8192 + i * 1024, lb + 8192 + i * 1024);
      waitvm<DEPTH - 8>();
      pub = max(pub, 2 * u + 2 - LAG); *FULL = pub;
      slot = (slot + 1 == NSLOT) ? 0 : slot + 1;
    }
    if (LAG >= 4) { waitvm<24>(); *FULL = 2 * NUNIT - 3; }
    if (LAG >= 3) { waitvm<16>(); *FULL = 2 * NUNIT - 2; }
    if (LAG >= 2) { waitvm<8>(); *FULL = 2 * NUNIT - 1; }
    waitvm<0>(); *FULL = 2 * NUNIT;
    STAMP(1);
    return;
  }

  const rsrc_t rw = mkrs(p.ws, W_END);
  const int cblk = 2 * (mem & 7) + (mem >> 3);
  const int j16 = jl & 15;
  const bool hasC = j16 < 14;
  const int fblk = xl * 14 + j16;
  const int ocol = xl * 64 + j16 * 4;
  unsigned tag = 0;

  v4u wo_r[4]; v2u w1_r[4], w2_r[4]; v2u xin;
  auto load_tail_weights = [&]() {
    const rsrc_t ro = mkrs(p.wo, 4096 * 512 * 2), r1 = mkrs(p.w1, 512 * 1792 * 2), r2 = mkrs(p.w2, 1792 * 512 * 2), rx = mkrs(p.x, 2048);
#pragma unroll
    for (int i = 0; i < 4; ++i) wo_r[i] = LD128(ro, (4 * n + i) * 1024 + g * 16, (kvh * 512 + w * 64) * 1024 + cblk * 64, AUX_NT);
#pragma unroll
    for (int i = 0; i < 4; ++i) w1_r[i] = LD64(r1, (4 * n + i) * 3584 + g * 8, hasC ? w * 64 * 3584 + fblk * 32 : (int)OOB, AUX_NT);
#pragma unroll
    for (int i = 0; i < 4; ++i) w2_r[i] = LD64(r2, lane < 56 ? (4 * lane + i) * 1024 : (int)OOB, w * 224 * 1024 + ocol * 2, AUX_NT);
    xin = LD64(rx, n * 8, b * 1024 + w * 128, 0);
  };

  if (w == 0) {
    {
      const v4u s4 = {SENT, SENT, SENT, SENT};
      ST128(s4, rw, lane * 16, W_PO + bid * 2048, AUX_SC1);
      ST128(s4, rw, lane * 16 + 1024, W_PO + bid * 2048, AUX_SC1);
      int o = (int)OOB;
      if (lane < 4) o = W_ML + bid * 128 + lane * 16;
      else if (lane < 12) o = W_AB + ((b * 8 + kvh) * 512 + cblk * 32) * 4 + (lane - 4) * 16;
      else if (lane < 14) { if (hasC) o = W_H1 + (b * 112 + fblk) * 128 + (lane - 12) * 16; }
      else if (lane < 18) o = W_QS + ((lane - 14) >> 1) * 8192 + (xl * 512 + jl * 16) * 2 + ((lane - 14) & 1) * 16;
      else if (lane < 20) o = W_MS + (lane - 18) * 4096 + (xl * 256 + jl * 8) * 2;
      ST128(s4, rw, o, 0, AUX_SC1);
    }
    const rsrc_t rc = mkrs(p.c, 2048), rx = mkrs(p.x, 2048), rm = mkrs(p.wmod, 512 * 3072 * 2);
    const int d0 = xl * 64 + jl * 2;
    const v4u c0 = LD128(rc, lane * 16, 0, 0), c1 = LD128(rc, lane * 16, 1024, 0);
    const v4u x0 = LD128(rx, lane * 16, 0, 0), x1 = LD128(rx, lane * 16, 1024, 0);
    const unsigned xd = LD32(rx, (g >> 1) * 1024, d0 * 2, 0);
    tag = LD32(rw, 0, W_EPOCH, AUX_SC1) + 1u;
    unsigned wsa[8], wca[8]; v4u wm[8];
#pragma unroll
    for (int i = 0; i < 8; ++i) { wsa[i] = LD32(rm, (lane * 8 + i) * 6144, d0 * 2, AUX_NT); wca[i] = LD32(rm, (lane * 8 + i) * 6144 + 1024, d0 * 2, AUX_NT); }
    __builtin_amdgcn_sched_barrier(0);
    unsigned s0[4], s1[4];
#pragma unroll
    for (int k = 0; k < 4; ++k) { s0[k] = cvtpk(silu_f(lo2f(c0[k])), silu_f(hi2f(c0[k]))); s1[k] = cvtpk(silu_f(lo2f(c1[k])), silu_f(hi2f(c1[k]))); }
    float sm0 = 0.f, sm1 = 0.f;
#pragma unroll
    for (int k = 0; k < 4; ++k) { sm0 += lo2f(x0[k]) + hi2f(x0[k]); sm1 += lo2f(x1[k]) + hi2f(x1[k]); }
    const float mean0 = wave_sum(sm0) * (1.f / 512.f), mean1 = wave_sum(sm1) * (1.f / 512.f);
    float q0 = 0.f, q1 = 0.f;
#pragma unroll
    for (int k = 0; k < 4; ++k) {
      float t = lo2f(x0[k]) - mean0; q0 += t * t; t = hi2f(x0[k]) - mean0; q0 += t * t;
      t = lo2f(x1[k]) - mean1; q1 += t * t; t = hi2f(x1[k]) - mean1; q1 += t * t;
    }
    q0 = wave_sum(q0); q1 = wave_sum(q1);
    const float mean = rbf(g < 2 ? mean0 : mean1), var = rbf((g < 2 ? q0 : q1) * (1.f / 512.f));
    const float isd = rcp(rbf(__builtin_amdgcn_sqrtf(rbf(var + 1e-5f))));
    const float ln = rbf(rbf(((g & 1) ? hi2f(xd) : lo2f(xd)) - mean) * isd);
    float v[8];
#pragma unroll
    for (int k = 0; k < 8; ++k) v[k] = 0.f;
#pragma unroll
    for (int pp = 0; pp < 4; ++pp) {
      const unsigned a0 = pk_lo(wsa[2 * pp], wsa[2 * pp + 1]), a1 = pk_hi(wsa[2 * pp], wsa[2 * pp + 1]);
      const unsigned e0 = pk_lo(wca[2 * pp], wca[2 * pp + 1]), e1 = pk_hi(wca[2 * pp], wca[2 * pp + 1]);
      v[0] = dot2(s0[pp], a0, v[0]); v[1] = dot2(s0[pp], e0, v[1]); v[2] = dot2(s0[pp], a1, v[2]); v[3] = dot2(s0[pp], e1, v[3]);
      v[4] = dot2(s1[pp], a0, v[4]); v[5] = dot2(s1[pp], e0, v[5]); v[6] = dot2(s1[pp], a1, v[6]); v[7] = dot2(s1[pp], e1, v[7]);
    }
#pragma unroll
    for (int k = 0; k < 4; ++k) v[k] = tr32(v[k], v[k + 4]);
    const float sa = rbf(row_sum(tr16(v[0], v[2]))), ca = rbf(row_sum(tr16(v[1], v[3])));
    const unsigned hb = cvtpk(rbf(ln * rbf(1.f + ca)) + sa, 0.f) & 0xffffu;
    auto hs = __builtin_amdgcn_permlane16_swap(hb, hb, false, false);
    unsigned hp = hs[0] | (hs[1] << 16);
    asm volatile("s_waitcnt vmcnt(0)" : "+v"(hp) :: "memory");
    if ((lane & 31) == 0) __hip_atomic_store((GAS u64*)(p.ws + W_HG + (d0 >> 1) * 16 + (lane >> 5) * 8), ((u64)tag << 32) | hp, RLX_AGENT);
    STAMP(1);
#pragma unroll
    for (int i = 0; i < 8; ++i) wm[i] = LD128(rm, (lane * 8 + i) * 6144, (1024 + xl * 256 + jl * 8) * 2, AUX_NT);
    load_tail_weights();
    const v2u r = gemv8(wm, s0, s1);
    ST64(r, rw, n == 0 ? (g >> 1) * 4096 + (g & 1) * 8 : (int)OOB, W_MS + (xl * 256 + jl * 8) * 2, AUX_SC1);
    STAMP(2);
  } else if (w < 3) {
    const rsrc_t rq = mkrs(p.wq, 512 * 4096 * 2);
    const int col0 = xl * 512 + jl * 16 + (w - 1) * 8;
    tag = LD32(rw, 0, W_EPOCH, AUX_SC1) + 1u;
    v4u wr[8];
#pragma unroll
    for (int i = 0; i < 8; ++i) wr[i] = LD128(rq, (lane * 8 + i) * 8192, col0 * 2, AUX_NT);
    __builtin_amdgcn_sched_barrier(0);
    asm volatile("" :: "v"(wr[7]));
    STAMP(1);
    unsigned h0[4], h1[4];
    for (unsigned spins = 0;;) {
      v4u t[4]; bool ok = true;
#pragma unroll
      for (int k = 0; k < 4; ++k) t[k] = LD128(rw, lane * 64 + k * 16, W_HG, AUX_SC1);
#pragma unroll
      for (int k = 0; k < 4; ++k) { ok &= (t[k][1] == tag) & (t[k][3] == tag); h0[k] = t[k][0]; h1[k] = t[k][2]; }
      if (__all(ok) || ++spins > SPIN_G) break;
      __builtin_amdgcn_s_sleep(4); asm volatile("" ::: "memory");
    }
    STAMP(2);
    const v2u r = gemv8(wr, h0, h1);
    ST64(r, rw, n == 0 ? (g >> 1) * 8192 + (g & 1) * 8 : (int)OOB, W_QS + col0 * 2, AUX_SC1);
    STAMP(3);
    if (w == 1) {
      v4u t;
      for (unsigned spins = 0;;) {
        __builtin_amdgcn_s_sleep(4); asm volatile("" ::: "memory");
        t = LD128(rw, lane * 16, W_QS + b * 8192 + kvh * 1024, AUX_SC1);
        if (__all(umax4(t) != SENT) || ++spins > SPIN_G) break;
      }
      *(LAS v4u*)(L + L_Q + lane * 16) = t;
      asm volatile("s_waitcnt lgkmcnt(0)" ::: "memory");
      *(volatile LAS unsigned*)(L + L_QRDY) = 1u;
      STAMP(4);
      load_tail_weights();
    } else {
      load_tail_weights();
      v4u t[4];
      for (unsigned spins = 0;;) {
        __builtin_amdgcn_s_sleep(32); asm volatile("" ::: "memory");
        unsigned mx = 0;
#pragma unroll
        for (int k = 0; k < 4; ++k) t[k] = LD128(rw, lane * 16 + k * 1024, W_MS + b * 4096, AUX_SC1);
#pragma unroll
        for (int k = 0; k < 4; ++k) mx = max(mx, umax4(t[k]));
        if (__all(mx != SENT) || ++spins > SPIN_G) break;
      }
#pragma unroll
      for (int k = 0; k < 4; ++k) *(LAS v4u*)(L + L_MOD + lane * 16 + k * 1024) = t[k];
      STAMP(4);
    }
  } else {
    if (w == 3) tag = LD32(rw, 0, W_EPOCH, AUX_SC1) + 1u;
    load_tail_weights();
    __builtin_amdgcn_sched_barrier(0);
  }

  unsigned wo_p[16], w1_p[8], w2_p[8];
#pragma unroll
  for (int pp = 0; pp < 2; ++pp) {
#pragma unroll
    for (int d = 0; d < 4; ++d) { wo_p[pp * 8 + 2 * d] = pk_lo(wo_r[2 * pp][d], wo_r[2 * pp + 1][d]); wo_p[pp * 8 + 2 * d + 1] = pk_hi(wo_r[2 * pp][d], wo_r[2 * pp + 1][d]); }
#pragma unroll
    for (int d = 0; d < 2; ++d) {
      w1_p[pp * 4 + 2 * d] = pk_lo(w1_r[2 * pp][d], w1_r[2 * pp + 1][d]); w1_p[pp * 4 + 2 * d + 1] = pk_hi(w1_r[2 * pp][d], w1_r[2 * pp + 1][d]);
      w2_p[pp * 4 + 2 * d] = pk_lo(w2_r[2 * pp][d], w2_r[2 * pp + 1][d]); w2_p[pp * 4 + 2 * d + 1] = pk_hi(w2_r[2 * pp][d], w2_r[2 * pp + 1][d]);
    }
  }

  int passv = (DRYRUN && w == 3) ? 0 : 1;
  for (;;) {
    asm volatile("" : "+v"(passv));
    const int pass = __builtin_amdgcn_readfirstlane(passv);
    f32x4 acc0 = {0.f, 0.f, 0.f, 0.f}, acc1 = {0.f, 0.f, 0.f, 0.f};
    float m = -INFINITY, l = 0.f;
    if (w >= 4) {
      const int c = w - 4;
      for (unsigned spins = 0; __builtin_amdgcn_readfirstlane(*(volatile LAS unsigned*)(L + L_QRDY)) == 0u && ++spins < SPIN_L;) __builtin_amdgcn_s_sleep(2);
      asm volatile("" ::: "memory");
      STAMP(1);
      v4u qf[4];
#pragma unroll
      for (int s = 0; s < 4; ++s) qf[s] = *(LAS v4u*)(L + L_Q + (n & 3) * 256 + s * 64 + g * 16);
      const int r7 = 4 * (g & 1) + (n >> 2);
      const int va0 = 8192 + (4 * g + (n >> 2)) * 256 + (((4 * c + ((n >> 1) & 1)) ^ (2 * r7)) * 16) + 8 * (n & 1), va1 = va0 ^ 32;
      unsigned full = 0; int slot = 0;
      constexpr float SC = 0.08838834764831845f;
#pragma unroll 1
      for (int u = 0; u < NUNIT; ++u) {
        LAS unsigned char* S = L + slot * 16384;
        for (unsigned spins = 0; full < (unsigned)(2 * u + 1);) { full = __builtin_amdgcn_readfirstlane(*FULL); if (full < (unsigned)(2 * u + 1)) __builtin_amdgcn_s_sleep(1); if (++spins > SPIN_L) full = SENT; }
        asm volatile("" ::: "memory");
        v4u kf[8];
#pragma unroll
        for (int i = 0; i < 8; ++i) kf[i] = *(LAS v4u*)(S + i * 1024 + lane * 16);
        __builtin_amdgcn_sched_barrier(0);
        f32x4 s0 = {0.f, 0.f, 0.f, 0.f}, s1 = {0.f, 0.f, 0.f, 0.f};
#pragma unroll
        for (int s = 0; s < 4; ++s) { s0 = mfma(kf[s], qf[s], s0); s1 = mfma(kf[4 + s], qf[s], s1); }
        float sc[8];
#pragma unroll
        for (int i = 0; i < 4; i += 2) {
          const unsigned u0 = cvtpk(s0[i], s0[i + 1]), u1 = cvtpk(s1[i], s1[i + 1]);
          const unsigned t0 = cvtpk(lo2f(u0) * SC, hi2f(u0) * SC), t1 = cvtpk(lo2f(u1) * SC, hi2f(u1) * SC);
          sc[i] = lo2f(t0); sc[i + 1] = hi2f(t0); sc[4 + i] = lo2f(t1); sc[4 + i + 1] = hi2f(t1);
        }
        const float bm = xg_max(fmaxf(fmaxf(fmaxf(sc[0], sc[1]), fmaxf(sc[2], sc[3])), fmaxf(fmaxf(sc[4], sc[5]), fmaxf(sc[6], sc[7]))));
        const float mn = fmaxf(m, bm);
        const float alpha = __builtin_amdgcn_exp2f((m - mn) * L2E);
        m = mn; acc0 *= alpha; acc1 *= alpha;
        const float mb = mn * L2E;
        float pr[8], ps = 0.f;
#pragma unroll
        for (int i = 0; i < 8; ++i) { pr[i] = __builtin_amdgcn_exp2f(sc[i] * L2E - mb); ps += pr[i]; }
        l = l * alpha + ps;
        v4u pf = {cvtpk(pr[0], pr[1]), cvtpk(pr[2], pr[3]), cvtpk(pr[4], pr[5]), cvtpk(pr[6], pr[7])};
        asm volatile("" : "+v"(pf), "+v"(acc0), "+v"(acc1), "+v"(l));
        for (unsigned spins = 0; full < (unsigned)(2 * u + 2);) { full = __builtin_amdgcn_readfirstlane(*FULL); if (++spins > SPIN_L) full = SENT; }
        asm volatile("" ::: "memory");
        const v2u a00 = __builtin_bit_cast(v2u, __builtin_amdgcn_ds_read_tr16_b64_v4i16((LAS s16x4*)(S + va0)));
        const v2u a01 = __builtin_bit_cast(v2u, __builtin_amdgcn_ds_read_tr16_b64_v4i16((LAS s16x4*)(S + va0 + 4096)));
        const v2u a10 = __builtin_bit_cast(v2u, __builtin_amdgcn_ds_read_tr16_b64_v4i16((LAS s16x4*)(S + va1)));
        const v2u a11 = __builtin_bit_cast(v2u, __builtin_amdgcn_ds_read_tr16_b64_v4i16((LAS s16x4*)(S + va1 + 4096)));
        asm volatile("s_waitcnt lgkmcnt(0)" ::: "memory");
        *(volatile LAS unsigned*)(L + L_FREE + c * 4) = (unsigned)(u + 1);
        acc0 = mfma(v4u{a00[0], a00[1], a01[0], a01[1]}, pf, acc0);
        acc1 = mfma(v4u{a10[0], a10[1], a11[0], a11[1]}, pf, acc1);
        slot = (slot + 1 == NSLOT) ? 0 : slot + 1;
      }
    }
    const rsrc_t rt = mkrs(p.ws, pass ? W_END : 0);
    if (w >= 4 || !pass) {
      l = xg_sum(l);
      f32x4 v = n < 4 ? acc0 : acc1;
      if (n >= 8) v = f32x4{m, l, m, l};
      int o = W_PO + bid * 2048 + (n & 3) * 512 + (w & 3) * 128 + (n >> 2) * 64 + g * 16;
      if (n >= 8) o = (n < 12 && g == 0 && w == 4) ? W_ML + bid * 128 + (n & 3) * 16 : (int)OOB;
      const v4u vu = __builtin_bit_cast(v4u, v);
      ST128((v4u{nosent(vu[0]), nosent(vu[1]), nosent(vu[2]), nosent(vu[3])}), rt, o, 0, AUX_SC1);
    }
    if (pass) { STAMP(5); bar(); }
    const v2u ga = *(LAS v2u*)(L + L_MOD + w * 128 + n * 8), sb = *(LAS v2u*)(L + L_MOD + 1024 + w * 128 + n * 8), cb = *(LAS v2u*)(L + L_MOD + 2048 + w * 128 + n * 8);
    const v2u gb = *(LAS v2u*)(L + L_MOD + 3072 + ocol * 2);
    {
      v4u o4[4]; v2u ml[4];
      const int po = W_PO + (bid & ~15) * 2048 + w * 256, mo = W_ML + (bid & ~15) * 128 + (w >> 1) * 16;
      for (unsigned spins = 0;;) {
        unsigned mx = 0;
#pragma unroll
        for (int i = 0; i < 4; ++i) { o4[i] = LD128(rt, (4 * i + g) * 2048 + n * 16, po, AUX_SC1); ml[i] = LD64(rt, (4 * i + g) * 128, mo, AUX_SC1); }
#pragma unroll
        for (int i = 0; i < 4; ++i) mx = max(max(mx, umax4(o4[i])), max(ml[i][0], ml[i][1]));
        if (__all(mx != SENT) || ++spins > SPIN_G) break;
        if (POLL_SLEEP) __builtin_amdgcn_s_sleep(POLL_SLEEP);
        asm volatile("" ::: "memory");
      }
      if (pass) STAMP(6);
      float mi[4], M;
#pragma unroll
      for (int i = 0; i < 4; ++i) mi[i] = __uint_as_float(ml[i][0]);
      M = xg_max(fmaxf(fmaxf(mi[0], mi[1]), fmaxf(mi[2], mi[3])));
      float Ls = 0.f; f32x4 o = {0.f, 0.f, 0.f, 0.f};
#pragma unroll
      for (int i = 0; i < 4; ++i) { const float e = __builtin_amdgcn_exp2f((mi[i] - M) * L2E); Ls += e * __uint_as_float(ml[i][1]); o += e * __builtin_bit_cast(f32x4, o4[i]); }
      const float inv = rcp(xg_sum(Ls));
      const unsigned p0 = cvtpk(xg_sum(o[0]) * inv, xg_sum(o[1]) * inv), p1 = cvtpk(xg_sum(o[2]) * inv, xg_sum(o[3]) * inv);
      float y[8];
#pragma unroll
      for (int k = 0; k < 8; ++k) y[k] = row_sum(dot2(p1, wo_p[8 + k], dot2(p0, wo_p[k], 0.f)));
      if (n < 2) *(LAS f32x4*)(L + L_RAB + w * 128 + g * 32 + n * 16) = n ? f32x4{y[4], y[5], y[6], y[7]} : f32x4{y[0], y[1], y[2], y[3]};
      if (pass) bar();
      if (w == 3) {
        f32x4 s = *(LAS f32x4*)(L + L_RAB + (lane >> 3) * 128 + (lane & 7) * 16);
#pragma unroll
        for (int k = 0; k < 4; ++k) { float t = s[k]; t += dpp<0x128>(t); s[k] = xg_sum(t); }
        const v4u su = __builtin_bit_cast(v4u, s);
        ST128((v4u{nosent(su[0]), nosent(su[1]), nosent(su[2]), nosent(su[3])}), rt, lane < 8 ? lane * 16 : (int)OOB, W_AB + ((b * 8 + kvh) * 512 + cblk * 32) * 4, AUX_SC1);
        if (pass) STAMP(7);
      }
    }
    {
      v4u a[2];
      for (unsigned spins = 0;;) {
#pragma unroll
        for (int i = 0; i < 2; ++i) a[i] = LD128(rt, (4 * i + g) * 2048 + n * 16, W_AB + b * 16384 + w * 256, AUX_SC1);
        if (__all(max(umax4(a[0]), umax4(a[1])) != SENT) || ++spins > SPIN_G) break;
        if (POLL_SLEEP) __builtin_amdgcn_s_sleep(POLL_SLEEP);
        asm volatile("" ::: "memory");
      }
      if (pass) STAMP(8);
      const f32x4 as = __builtin_bit_cast(f32x4, a[0]) + __builtin_bit_cast(f32x4, a[1]);
      float x1[4];
      x1[0] = rbf(lo2f(xin[0]) + rbf(lo2f(ga[0]) * rbf(xg_sum(as[0])))); x1[1] = rbf(hi2f(xin[0]) + rbf(hi2f(ga[0]) * rbf(xg_sum(as[1]))));
      x1[2] = rbf(lo2f(xin[1]) + rbf(lo2f(ga[1]) * rbf(xg_sum(as[2])))); x1[3] = rbf(hi2f(xin[1]) + rbf(hi2f(ga[1]) * rbf(xg_sum(as[3]))));
      const float mw = row_sum((x1[0] + x1[1]) + (x1[2] + x1[3])) * (1.f / 64.f);
      float q = 0.f;
#pragma unroll
      for (int k = 0; k < 4; ++k) { const float t = x1[k] - mw; q += t * t; }
      q = row_sum(q);
      if (lane == 0) *(LAS f32x2*)(L + L_ST + w * 8) = f32x2{mw, q};
      if (w == xl && lane == j16) *(LAS f32x4*)(L + L_X1) = f32x4{x1[0], x1[1], x1[2], x1[3]};
      if (pass) bar();
      f32x4 st[4];
#pragma unroll
      for (int k = 0; k < 4; ++k) st[k] = *(LAS f32x4*)(L + L_ST + k * 16);
      const float mu = (((st[0][0] + st[0][2]) + (st[1][0] + st[1][2])) + ((st[2][0] + st[2][2]) + (st[3][0] + st[3][2]))) * 0.125f;
      float m2 = 0.f, dv = 0.f;
#pragma unroll
      for (int k = 0; k < 4; ++k) { m2 += st[k][1] + st[k][3]; const float t0 = st[k][0] - mu, t1 = st[k][2] - mu; dv += t0 * t0 + t1 * t1; }
      const float mean = rbf(mu), var = rbf((m2 + 64.f * dv) * (1.f / 512.f));
      const float isd = rcp(rbf(__builtin_amdgcn_sqrtf(rbf(var + 1e-5f))));
      const float h0 = rbf(rbf(rbf(x1[0] - mean) * isd) * rbf(1.f + lo2f(cb[0]))) + lo2f(sb[0]), h1 = rbf(rbf(rbf(x1[1] - mean) * isd) * rbf(1.f + hi2f(cb[0]))) + hi2f(sb[0]);
      const float h2 = rbf(rbf(rbf(x1[2] - mean) * isd) * rbf(1.f + lo2f(cb[1]))) + lo2f(sb[1]), h3 = rbf(rbf(rbf(x1[3] - mean) * isd) * rbf(1.f + hi2f(cb[1]))) + hi2f(sb[1]);
      const unsigned p0 = cvtpk(h0, h1), p1 = cvtpk(h2, h3);
      f32x4 y;
#pragma unroll
      for (int k = 0; k < 4; ++k) y[k] = row_sum(dot2(p1, w1_p[4 + k], dot2(p0, w1_p[k], 0.f)));
      if (n == 0) *(LAS f32x4*)(L + L_RC + w * 64 + g * 16) = y;
      if (pass) bar();
      if (w == 3) {
        const f32x2 s = *(LAS f32x2*)(L + L_RC + (lane >> 3) * 64 + (lane & 7) * 8);
        float t0 = s[0], t1 = s[1];
        t0 += dpp<0x128>(t0); t1 += dpp<0x128>(t1);
        t0 = xg_sum(t0); t1 = xg_sum(t1);
        const unsigned d = nosent(cvtpk(silu_f(rbf(t0)), silu_f(rbf(t1))));
        const int di = (int)d;
        const v4u o = {d, (unsigned)__builtin_amdgcn_mov_dpp(di, 0x55, 0xf, 0xf, true), (unsigned)__builtin_amdgcn_mov_dpp(di, 0xAA, 0xf, 0xf, true), (unsigned)__builtin_amdgcn_mov_dpp(di, 0xFF, 0xf, 0xf, true)};
        ST128(o, rt, (lane == 0 || lane == 4) && hasC ? lane * 4 : (int)OOB, W_H1 + (b * 112 + fblk) * 128, AUX_SC1);
        if (pass) STAMP(9);
      }
    }
    {
      v2u hv;
      const int e = w * 56 + lane;
      for (unsigned spins = 0;;) {
        hv = LD64(rt, lane < 56 ? (e >> 2) * 128 + (e & 3) * 8 : (int)OOB, W_H1 + b * 112 * 128, AUX_SC1);
        if (__all(max(hv[0], hv[1]) != SENT) || ++spins > SPIN_G) break;
        if (POLL_SLEEP) __builtin_amdgcn_s_sleep(POLL_SLEEP);
        asm volatile("" ::: "memory");
      }
      if (pass) STAMP(10);
      float y[4];
#pragma unroll
      for (int k = 0; k < 4; ++k) y[k] = dot2(hv[1], w2_p[4 + k], dot2(hv[0], w2_p[k], 0.f));
      const float t = row_sum(tr16(tr32(y[0], y[2]), tr32(y[1], y[3])));
      if (n == 0) *(LAS float*)(L + L_RD + g * 32 + w * 4) = t;
      if (pass) bar();
      if (w == 3) {
        const f32x4 r0 = *(LAS f32x4*)(L + L_RD + (lane & 3) * 32), r1 = *(LAS f32x4*)(L + L_RD + (lane & 3) * 32 + 16);
        const float y2 = ((r0[0] + r0[1]) + (r0[2] + r0[3])) + ((r1[0] + r1[1]) + (r1[2] + r1[3]));
        const float xv = *(LAS float*)(L + L_X1 + (lane & 3) * 4);
        const unsigned gw = (lane & 2) ? gb[1] : gb[0];
        const float gv = (lane & 1) ? hi2f(gw) : lo2f(gw);
        const float r = xv + rbf(gv * rbf(y2));
        const v2u o = {cvtpk(dpp<0x00>(r), dpp<0x55>(r)), cvtpk(dpp<0xAA>(r), dpp<0xFF>(r))};
        if (pass && lane == 0) *(GAS v2u*)(p.out + b * 512 + ocol) = o;
        __builtin_amdgcn_raw_buffer_store_b32(tag, rt, (lane == 0 && bid == 0) ? 0 : (int)OOB, W_EPOCH, AUX_SC1);
        if (pass) STAMP(11);
      }
    }
    if (pass) break;
    passv = 1;
  }
}

extern "C" void kernel_launch(void* const* d_in, const int* in_sizes, int n_in, void* d_out, int out_size, void* d_ws, size_t ws_size, hipStream_t stream) {
  Params p;
  p.x = (const u16*)d_in[0]; p.K = (const u16*)d_in[1]; p.V = (const u16*)d_in[2]; p.wq = (const u16*)d_in[3]; p.wo = (const u16*)d_in[4];
  p.w1 = (const u16*)d_in[6]; p.w2 = (const u16*)d_in[7]; p.c = (const u16*)d_in[8]; p.wmod = (const u16*)d_in[9];
  p.out = (u16*)d_out; p.ws = (unsigned char*)d_ws;
  (void)hipFuncSetAttribute((const void*)adaln_block_kernel, hipFuncAttributeMaxDynamicSharedMemorySize, L_END);
  hipLaunchKernelGGL(adaln_block_kernel, dim3(NWG), dim3(NTHR), L_END, stream, p);
}
```

```cpp
#define KPOL "sc0 sc1 nt"
#define VPOL "sc0 sc1 nt"
#include <hip/hip_runtime.h>
#include <hip/hip_bf16.h>
#include <stdint.h>

typedef unsigned short u16;
typedef unsigned long long u64;
typedef unsigned v4u __attribute__((ext_vector_type(4)));
typedef unsigned v2u __attribute__((ext_vector_type(2)));
typedef float f32x4 __attribute__((ext_vector_type(4)));
typedef float f32x2 __attribute__((ext_vector_type(2)));
typedef short bf16x8 __attribute__((ext_vector_type(8)));
typedef short s16x4 __attribute__((ext_vector_type(4)));
typedef __bf16 bf16x2_t __attribute__((ext_vector_type(2)));
typedef __amdgpu_buffer_rsrc_t rsrc_t;
#define GAS __attribute__((address_space(1)))
#define LAS __attribute__((address_space(3)))
#define RLX_AGENT __ATOMIC_RELAXED, __HIP_MEMORY_SCOPE_AGENT

#define NSLOT 9
#define DEPTH 32
#define DRYRUN 1
#define POLL_SLEEP 0

constexpr int NWG = 256, NTHR = 576, NUNIT = 64;
constexpr unsigned SENT = 0xFFFFFFFFu, OOB = 0x7FFFFFF0u;
constexpr int AUX_NT = 2, AUX_SC1 = 16;
constexpr int W_EPOCH = 0, W_HG = 0x1000, W_QS = 0x2000, W_MS = 0x6000, W_AB = 0x8000, W_H1 = 0x10000, W_ML = 0x18000, W_PO = 0x20000, W_END = 0xA0000;
constexpr int W_STAMP = 0x100000;
constexpr int L_CTRL = NSLOT * 16384, L_FULL = L_CTRL, L_QRDY = L_CTRL + 4, L_FREE = L_CTRL + 16, L_Q = L_CTRL + 64, L_MOD = L_Q + 1024,
              L_RAB = L_MOD + 4096, L_ST = L_RAB + 1024, L_RC = L_ST + 64, L_RD = L_RC + 512, L_X1 = L_RD + 128, L_END = L_X1 + 16;
static_assert(L_END <= 160 * 1024, "LDS");
constexpr unsigned SPIN_G = 1u << 17, SPIN_L = 1u << 22;

#define STAMP(k) do {} while (0)

__device__ __forceinline__ float lo2f(unsigned u) { return __builtin_bit_cast(float, u << 16); }
__device__ __forceinline__ float hi2f(unsigned u) { return __builtin_bit_cast(float, u & 0xffff0000u); }
__device__ __forceinline__ unsigned cvtpk(float lo, float hi) { f32x2 v = {lo, hi}; return __builtin_bit_cast(unsigned, __builtin_convertvector(v, bf16x2_t)); }
__device__ __forceinline__ float rbf(float x) { return lo2f(cvtpk(x, x)); }
__device__ __forceinline__ float rcp(float x) { return __builtin_amdgcn_rcpf(x); }
__device__ __forceinline__ float silu_f(float x) { return x * rcp(1.f + __builtin_amdgcn_exp2f(x * -1.4426950408889634f)); }
__device__ __forceinline__ f32x4 mfma(v4u a, v4u b, f32x4 c) {
  return __builtin_amdgcn_mfma_f32_16x16x32_bf16(__builtin_bit_cast(bf16x8, a), __builtin_bit_cast(bf16x8, b), c, 0, 0, 0);
}
__device__ __forceinline__ float dot2(unsigned a, unsigned b, float c) {
  return __builtin_amdgcn_fdot2_f32_bf16(__builtin_bit_cast(bf16x2_t, a), __builtin_bit_cast(bf16x2_t, b), c, false);
}
__device__ __forceinline__ unsigned pk_lo(unsigned r0, unsigned r1) { return __builtin_amdgcn_perm(r1, r0, 0x05040100u); }
__device__ __forceinline__ unsigned pk_hi(unsigned r0, unsigned r1) { return __builtin_amdgcn_perm(r1, r0, 0x07060302u); }
__device__ __forceinline__ unsigned nosent(unsigned d) { return d < 0xFFFFFFFEu ? d : 0xFFFFFFFEu; }
template <int CTRL> __device__ __forceinline__ float dpp(float x) {
  return __builtin_bit_cast(float, __builtin_amdgcn_mov_dpp(__builtin_bit_cast(int, x), CTRL, 0xf, 0xf, true));
}
__device__ __forceinline__ float row_sum(float x) { x += dpp<0x128>(x); x += dpp<0x124>(x); x += dpp<0x122>(x); x += dpp<0x121>(x); return x; }
__device__ __forceinline__ float tr32(float a, float b) {
  auto s = __builtin_amdgcn_permlane32_swap(__float_as_uint(a), __float_as_uint(b), false, false);
  return __uint_as_float(s[0]) + __uint_as_float(s[1]);
}
__device__ __forceinline__ float tr16(float a, float b) {
  auto s = __builtin_amdgcn_permlane16_swap(__float_as_uint(a), __float_as_uint(b), false, false);
  return __uint_as_float(s[0]) + __uint_as_float(s[1]);
}
__device__ __forceinline__ float xg_sum(float x) { x = tr16(x, x); return tr32(x, x); }
__device__ __forceinline__ float xg_max(float x) {
  auto s = __builtin_amdgcn_permlane16_swap(__float_as_uint(x), __float_as_uint(x), false, false);
  x = fmaxf(__uint_as_float(s[0]), __uint_as_float(s[1]));
  auto t = __builtin_amdgcn_permlane32_swap(__float_as_uint(x), __float_as_uint(x), false, false);
  return fmaxf(__uint_as_float(t[0]), __uint_as_float(t[1]));
}
__device__ __forceinline__ float wave_sum(float x) { return xg_sum(row_sum(x)); }
__device__ __forceinline__ unsigned umax4(v4u v) { return max(max(v[0], v[1]), max(v[2], v[3])); }
template <int N> __device__ __forceinline__ void waitvm() { asm volatile("s_waitcnt vmcnt(%0)" :: "n"(N) : "memory"); }
__device__ __forceinline__ void bar() { asm volatile("s_waitcnt lgkmcnt(0)\n\ts_barrier" ::: "memory"); }
__device__ __forceinline__ rsrc_t mkrs(const void* p, unsigned bytes) { return __builtin_amdgcn_make_buffer_rsrc((void*)p, 0, bytes, 0x00020000); }
__device__ __forceinline__ void dmak(rsrc_t rs, int voff, int soff, unsigned lds) {
  asm volatile("s_mov_b32 m0, %3\n\ts_nop 0\n\tbuffer_load_dwordx4 %0, %1, %2 offen " KPOL " lds" :: "v"(voff), "s"(rs), "s"(soff), "s"(lds) : "memory");
}
__device__ __forceinline__ void dmav(rsrc_t rs, int voff, int soff, unsigned lds) {
  asm volatile("s_mov_b32 m0, %3\n\ts_nop 0\n\tbuffer_load_dwordx4 %0, %1, %2 offen " VPOL " lds" :: "v"(voff), "s"(rs), "s"(soff), "s"(lds) : "memory");
}
#define LD128(rs, vo, so, aux) __builtin_amdgcn_raw_buffer_load_b128(rs, vo, so, aux)
#define LD64(rs, vo, so, aux) __builtin_amdgcn_raw_buffer_load_b64(rs, vo, so, aux)
#define LD32(rs, vo, so, aux) __builtin_amdgcn_raw_buffer_load_b32(rs, vo, so, aux)
#define ST128(v, rs, vo, so, aux) __builtin_amdgcn_raw_buffer_store_b128(v, rs, vo, so, aux)
#define ST64(v, rs, vo, so, aux) __builtin_amdgcn_raw_buffer_store_b64(v, rs, vo, so, aux)

__device__ __forceinline__ v2u gemv8(const v4u (&wr)[8], const unsigned (&a0)[4], const unsigned (&a1)[4]) {
  float v[16];
#pragma unroll
  for (int k = 0; k < 16; ++k) v[k] = 0.f;
#pragma unroll
  for (int pp = 0; pp < 4; ++pp)
#pragma unroll
    for (int d = 0; d < 4; ++d) {
      const unsigned lo = pk_lo(wr[2 * pp][d], wr[2 * pp + 1][d]), hi = pk_hi(wr[2 * pp][d], wr[2 * pp + 1][d]);
      v[2 * d] = dot2(a0[pp], lo, v[2 * d]); v[2 * d + 1] = dot2(a0[pp], hi, v[2 * d + 1]);
      v[8 + 2 * d] = dot2(a1[pp], lo, v[8 + 2 * d]); v[8 + 2 * d + 1] = dot2(a1[pp], hi, v[8 + 2 * d + 1]);
    }
#pragma unroll
  for (int k = 0; k < 8; ++k) v[k] = tr32(v[k], v[k + 8]);
#pragma unroll
  for (int k = 0; k < 4; ++k) v[k] = row_sum(tr16(v[k], v[k + 4]));
  return v2u{nosent(cvtpk(v[0], v[1])), nosent(cvtpk(v[2], v[3]))};
}

struct Params { const u16 *x, *K, *V, *wq, *wo, *w1, *w2, *c, *wmod; u16* out; unsigned char* ws; };

__global__ void __launch_bounds__(NTHR, 3) adaln_block_kernel(Params p) {
  extern __shared__ __attribute__((aligned(16))) unsigned char smem[];
  LAS unsigned char* L = (LAS unsigned char*)smem;
  const int tid = threadIdx.x, lane = tid & 63, w = __builtin_amdgcn_readfirstlane(tid >> 6);
  const int bid = blockIdx.x, xl = bid & 7, jl = bid >> 3, b = bid >> 7, kvh = (bid >> 4) & 7, mem = bid & 15;
  const int g = lane >> 4, n = lane & 15;
  volatile LAS unsigned* FULL = (volatile LAS unsigned*)(L + L_FULL);
  constexpr float L2E = 1.4426950408889634f;
  STAMP(0);
  if (tid < 8) ((LAS unsigned*)(L + L_CTRL))[tid] = 0u;
  bar();

  if (w == 8) {
    __builtin_amdgcn_s_setprio(2);
    const rsrc_t rk = mkrs(p.K + (size_t)bid * (NUNIT * 4096), NUNIT * 8192), rv = mkrs(p.V + (size_t)bid * (NUNIT * 4096), NUNIT * 8192);
    const int kv = n * 256 + g * 16, ve = g * 256 + ((n ^ (2 * g)) * 16), vo = g * 256 + ((n ^ (8 + 2 * g)) * 16);
    unsigned freed = 0; int slot = 0, pub = 0;
    constexpr int LAG = DEPTH / 8 - 1;
#pragma unroll 1
    for (int u = 0; u < NUNIT; ++u) {
      if (u >= NSLOT && freed < (unsigned)(u - NSLOT + 1)) {
        unsigned spins = 0; bool drained = false;
        for (;;) {
          const v4u f = *(volatile LAS v4u*)(L + L_FREE);
          freed = __builtin_amdgcn_readfirstlane(min(min(f[0], f[1]), min(f[2], f[3])));
          if (freed >= (unsigned)(u - NSLOT + 1)) break;
          if (++spins > SPIN_L) { freed = SENT; break; }
          if (!drained) { waitvm<0>(); pub = 2 * u; *FULL = pub; drained = true; STAMP(2); }
          __builtin_amdgcn_s_sleep(1);
        }
      }
      const unsigned lb = slot * 16384;
#pragma unroll
      for (int i = 0; i < 8; ++i) dmak(rk, kv, u * 8192 + (i >> 2) * 4096 + (i & 3) * 64, lb + i * 1024);
      waitvm<DEPTH - 8>();
      pub = max(pub, 2 * u + 1 - LAG); *FULL = pub;
#pragma unroll
      for (int i = 0; i < 8; ++i) dmav(rv, (i & 1) ? vo : ve, u * 8192 + i * 1024, lb + 8192 + i * 1024);
      waitvm<DEPTH - 8>();
      pub = max(pub, 2 * u + 2 - LAG); *FULL = pub;
      slot = (slot + 1 == NSLOT) ? 0 : slot + 1;
    }
    if (LAG >= 4) { waitvm<24>(); *FULL = 2 * NUNIT - 3; }
    if (LAG >= 3) { waitvm<16>(); *FULL = 2 * NUNIT - 2; }
    if (LAG >= 2) { waitvm<8>(); *FULL = 2 * NUNIT - 1; }
    waitvm<0>(); *FULL = 2 * NUNIT;
    STAMP(1);
    return;
  }

  const rsrc_t rw = mkrs(p.ws, W_END);
  const int cblk = 2 * (mem & 7) + (mem >> 3);
  const int j16 = jl & 15;
  const bool hasC = j16 < 14;
  const int fblk = xl * 14 + j16;
  const int ocol = xl * 64 + j16 * 4;
  unsigned tag = 0;

  v4u wo_r[4]; v2u w1_r[4], w2_r[4]; v2u xin;
  auto load_tail_weights = [&]() {
    const rsrc_t ro = mkrs(p.wo, 4096 * 512 * 2), r1 = mkrs(p.w1, 512 * 1792 * 2), r2 = mkrs(p.w2, 1792 * 512 * 2), rx = mkrs(p.x, 2048);
#pragma unroll
    for (int i = 0; i < 4; ++i) wo_r[i] = LD128(ro, (4 * n + i) * 1024 + g * 16, (kvh * 512 + w * 64) * 1024 + cblk * 64, AUX_NT);
#pragma unroll
    for (int i = 0; i < 4; ++i) w1_r[i] = LD64(r1, (4 * n + i) * 3584 + g * 8, hasC ? w * 64 * 3584 + fblk * 32 : (int)OOB, AUX_NT);
#pragma unroll
    for (int i = 0; i < 4; ++i) w2_r[i] = LD64(r2, lane < 56 ? (4 * lane + i) * 1024 : (int)OOB, w * 224 * 1024 + ocol * 2, AUX_NT);
    xin = LD64(rx, n * 8, b * 1024 + w * 128, 0);
  };

  if (w == 0) {
    {
      const v4u s4 = {SENT, SENT, SENT, SENT};
      ST128(s4, rw, lane * 16, W_PO + bid * 2048, AUX_SC1);
      ST128(s4, rw, lane * 16 + 1024, W_PO + bid * 2048, AUX_SC1);
      int o = (int)OOB;
      if (lane < 4) o = W_ML + bid * 128 + lane * 16;
      else if (lane < 12) o = W_AB + ((b * 8 + kvh) * 512 + cblk * 32) * 4 + (lane - 4) * 16;
      else if (lane < 14) { if (hasC) o = W_H1 + (b * 112 + fblk) * 128 + (lane - 12) * 16; }
      else if (lane < 18) o = W_QS + ((lane - 14) >> 1) * 8192 + (xl * 512 + jl * 16) * 2 + ((lane - 14) & 1) * 16;
      else if (lane < 20) o = W_MS + (lane - 18) * 4096 + (xl * 256 + jl * 8) * 2;
      ST128(s4, rw, o, 0, AUX_SC1);
    }
    const rsrc_t rc = mkrs(p.c, 2048), rx = mkrs(p.x, 2048), rm = mkrs(p.wmod, 512 * 3072 * 2);
    const int d0 = xl * 64 + jl * 2;
    const v4u c0 = LD128(rc, lane * 16, 0, 0), c1 = LD128(rc, lane * 16, 1024, 0);
    const v4u x0 = LD128(rx, lane * 16, 0, 0), x1 = LD128(rx, lane * 16, 1024, 0);
    const unsigned xd = LD32(rx, (g >> 1) * 1024, d0 * 2, 0);
    tag = LD32(rw, 0, W_EPOCH, AUX_SC1) + 1u;
    unsigned wsa[8], wca[8]; v4u wm[8];
#pragma unroll
    for (int i = 0; i < 8; ++i) { wsa[i] = LD32(rm, (lane * 8 + i) * 6144, d0 * 2, AUX_NT); wca[i] = LD32(rm, (lane * 8 + i) * 6144 + 1024, d0 * 2, AUX_NT); }
    __builtin_amdgcn_sched_barrier(0);
    unsigned s0[4], s1[4];
#pragma unroll
    for (int k = 0; k < 4; ++k) { s0[k] = cvtpk(silu_f(lo2f(c0[k])), silu_f(hi2f(c0[k]))); s1[k] = cvtpk(silu_f(lo2f(c1[k])), silu_f(hi2f(c1[k]))); }
    float sm0 = 0.f, sm1 = 0.f;
#pragma unroll
    for (int k = 0; k < 4; ++k) { sm0 += lo2f(x0[k]) + hi2f(x0[k]); sm1 += lo2f(x1[k]) + hi2f(x1[k]); }
    const float mean0 = wave_sum(sm0) * (1.f / 512.f), mean1 = wave_sum(sm1) * (1.f / 512.f);
    float q0 = 0.f, q1 = 0.f;
#pragma unroll
    for (int k = 0; k < 4; ++k) {
      float t = lo2f(x0[k]) - mean0; q0 += t * t; t = hi2f(x0[k]) - mean0; q0 += t * t;
      t = lo2f(x1[k]) - mean1; q1 += t * t; t = hi2f(x1[k]) - mean1; q1 += t * t;
    }
    q0 = wave_sum(q0); q1 = wave_sum(q1);
    const float mean = rbf(g < 2 ? mean0 : mean1), var = rbf((g < 2 ? q0 : q1) * (1.f / 512.f));
    const float isd = rcp(rbf(__builtin_amdgcn_sqrtf(rbf(var + 1e-5f))));
    const float ln = rbf(rbf(((g & 1) ? hi2f(xd) : lo2f(xd)) - mean) * isd);
    float v[8];
#pragma unroll
    for (int k = 0; k < 8; ++k) v[k] = 0.f;
#pragma unroll
    for (int pp = 0; pp < 4; ++pp) {
      const unsigned a0 = pk_lo(wsa[2 * pp], wsa[2 * pp + 1]), a1 = pk_hi(wsa[2 * pp], wsa[2 * pp + 1]);
      const unsigned e0 = pk_lo(wca[2 * pp], wca[2 * pp + 1]), e1 = pk_hi(wca[2 * pp], wca[2 * pp + 1]);
      v[0] = dot2(s0[pp], a0, v[0]); v[1] = dot2(s0[pp], e0, v[1]); v[2] = dot2(s0[pp], a1, v[2]); v[3] = dot2(s0[pp], e1, v[3]);
      v[4] = dot2(s1[pp], a0, v[4]); v[5] = dot2(s1[pp], e0, v[5]); v[6] = dot2(s1[pp], a1, v[6]); v[7] = dot2(s1[pp], e1, v[7]);
    }
#pragma unroll
    for (int k = 0; k < 4; ++k) v[k] = tr32(v[k], v[k + 4]);
    const float sa = rbf(row_sum(tr16(v[0], v[2]))), ca = rbf(row_sum(tr16(v[1], v[3])));
    const unsigned hb = cvtpk(rbf(ln * rbf(1.f + ca)) + sa, 0.f) & 0xffffu;
    auto hs = __builtin_amdgcn_permlane16_swap(hb, hb, false, false);
    unsigned hp = hs[0] | (hs[1] << 16);
    asm volatile("s_waitcnt vmcnt(0)" : "+v"(hp) :: "memory");
    if ((lane & 31) == 0) __hip_atomic_store((GAS u64*)(p.ws + W_HG + (d0 >> 1) * 16 + (lane >> 5) * 8), ((u64)tag << 32) | hp, RLX_AGENT);
    STAMP(1);
#pragma unroll
    for (int i = 0; i < 8; ++i) wm[i] = LD128(rm, (lane * 8 + i) * 6144, (1024 + xl * 256 + jl * 8) * 2, AUX_NT);
    load_tail_weights();
    const v2u r = gemv8(wm, s0, s1);
    ST64(r, rw, n == 0 ? (g >> 1) * 4096 + (g & 1) * 8 : (int)OOB, W_MS + (xl * 256 + jl * 8) * 2, AUX_SC1);
    STAMP(2);
  } else if (w < 3) {
    const rsrc_t rq = mkrs(p.wq, 512 * 4096 * 2);
    const int col0 = xl * 512 + jl * 16 + (w - 1) * 8;
    tag = LD32(rw, 0, W_EPOCH, AUX_SC1) + 1u;
    v4u wr[8];
#pragma unroll
    for (int i = 0; i < 8; ++i) wr[i] = LD128(rq, (lane * 8 + i) * 8192, col0 * 2, AUX_NT);
    __builtin_amdgcn_sched_barrier(0);
    asm volatile("" :: "v"(wr[7]));
    STAMP(1);
    unsigned h0[4], h1[4];
    for (unsigned spins = 0;;) {
      v4u t[4]; bool ok = true;
#pragma unroll
      for (int k = 0; k < 4; ++k) t[k] = LD128(rw, lane * 64 + k * 16, W_HG, AUX_SC1);
#pragma unroll
      for (int k = 0; k < 4; ++k) { ok &= (t[k][1] == tag) & (t[k][3] == tag); h0[k] = t[k][0]; h1[k] = t[k][2]; }
      if (__all(ok) || ++spins > SPIN_G) break;
      __builtin_amdgcn_s_sleep(4); asm volatile("" ::: "memory");
    }
    STAMP(2);
    const v2u r = gemv8(wr, h0, h1);
    ST64(r, rw, n == 0 ? (g >> 1) * 8192 + (g & 1) * 8 : (int)OOB, W_QS + col0 * 2, AUX_SC1);
    STAMP(3);
    if (w == 1) {
      v4u t;
      for (unsigned spins = 0;;) {
        __builtin_amdgcn_s_sleep(4); asm volatile("" ::: "memory");
        t = LD128(rw, lane * 16, W_QS + b * 8192 + kvh * 1024, AUX_SC1);
        if (__all(umax4(t) != SENT) || ++spins > SPIN_G) break;
      }
      *(LAS v4u*)(L + L_Q + lane * 16) = t;
      asm volatile("s_waitcnt lgkmcnt(0)" ::: "memory");
      *(volatile LAS unsigned*)(L + L_QRDY) = 1u;
      STAMP(4);
      load_tail_weights();
    } else {
      load_tail_weights();
      v4u t[4];
      for (unsigned spins = 0;;) {
        __builtin_amdgcn_s_sleep(32); asm volatile("" ::: "memory");
        unsigned mx = 0;
#pragma unroll
        for (int k = 0; k < 4; ++k) t[k] = LD128(rw, lane * 16 + k * 1024, W_MS + b * 4096, AUX_SC1);
#pragma unroll
        for (int k = 0; k < 4; ++k) mx = max(mx, umax4(t[k]));
        if (__all(mx != SENT) || ++spins > SPIN_G) break;
      }
#pragma unroll
      for (int k = 0; k < 4; ++k) *(LAS v4u*)(L + L_MOD + lane * 16 + k * 1024) = t[k];
      STAMP(4);
    }
  } else {
    if (w == 3) tag = LD32(rw, 0, W_EPOCH, AUX_SC1) + 1u;
    load_tail_weights();
    __builtin_amdgcn_sched_barrier(0);
  }

  unsigned wo_p[16], w1_p[8], w2_p[8];
#pragma unroll
  for (int pp = 0; pp < 2; ++pp) {
#pragma unroll
    for (int d = 0; d < 4; ++d) { wo_p[pp * 8 + 2 * d] = pk_lo(wo_r[2 * pp][d], wo_r[2 * pp + 1][d]); wo_p[pp * 8 + 2 * d + 1] = pk_hi(wo_r[2 * pp][d], wo_r[2 * pp + 1][d]); }
#pragma unroll
    for (int d = 0; d < 2; ++d) {
      w1_p[pp * 4 + 2 * d] = pk_lo(w1_r[2 * pp][d], w1_r[2 * pp + 1][d]); w1_p[pp * 4 + 2 * d + 1] = pk_hi(w1_r[2 * pp][d], w1_r[2 * pp + 1][d]);
      w2_p[pp * 4 + 2 * d] = pk_lo(w2_r[2 * pp][d], w2_r[2 * pp + 1][d]); w2_p[pp * 4 + 2 * d + 1] = pk_hi(w2_r[2 * pp][d], w2_r[2 * pp + 1][d]);
    }
  }

  int passv = (DRYRUN && w == 3) ? 0 : 1;
  for (;;) {
    asm volatile("" : "+v"(passv));
    const int pass = __builtin_amdgcn_readfirstlane(passv);
    f32x4 acc0 = {0.f, 0.f, 0.f, 0.f}, acc1 = {0.f, 0.f, 0.f, 0.f};
    float m = -INFINITY, l = 0.f;
    if (w >= 4) {
      const int c = w - 4;
      for (unsigned spins = 0; __builtin_amdgcn_readfirstlane(*(volatile LAS unsigned*)(L + L_QRDY)) == 0u && ++spins < SPIN_L;) __builtin_amdgcn_s_sleep(2);
      asm volatile("" ::: "memory");
      STAMP(1);
      v4u qf[4];
#pragma unroll
      for (int s = 0; s < 4; ++s) qf[s] = *(LAS v4u*)(L + L_Q + (n & 3) * 256 + s * 64 + g * 16);
      const int r7 = 4 * (g & 1) + (n >> 2);
      const int va0 = 8192 + (4 * g + (n >> 2)) * 256 + (((4 * c + ((n >> 1) & 1)) ^ (2 * r7)) * 16) + 8 * (n & 1), va1 = va0 ^ 32;
      unsigned full = 0; int slot = 0;
      constexpr float SC = 0.08838834764831845f;
#pragma unroll 1
      for (int u = 0; u < NUNIT; ++u) {
        LAS unsigned char* S = L + slot * 16384;
        for (unsigned spins = 0; full < (unsigned)(2 * u + 1);) { full = __builtin_amdgcn_readfirstlane(*FULL); if (full < (unsigned)(2 * u + 1)) __builtin_amdgcn_s_sleep(1); if (++spins > SPIN_L) full = SENT; }
        asm volatile("" ::: "memory");
        v4u kf[8];
#pragma unroll
        for (int i = 0; i < 8; ++i) kf[i] = *(LAS v4u*)(S + i * 1024 + lane * 16);
        __builtin_amdgcn_sched_barrier(0);
        f32x4 s0 = {0.f, 0.f, 0.f, 0.f}, s1 = {0.f, 0.f, 0.f, 0.f};
#pragma unroll
        for (int s = 0; s < 4; ++s) { s0 = mfma(kf[s], qf[s], s0); s1 = mfma(kf[4 + s], qf[s], s1); }
        float sc[8];
#pragma unroll
        for (int i = 0; i < 4; i += 2) {
          const unsigned u0 = cvtpk(s0[i], s0[i + 1]), u1 = cvtpk(s1[i], s1[i + 1]);
          const unsigned t0 = cvtpk(lo2f(u0) * SC, hi2f(u0) * SC), t1 = cvtpk(lo2f(u1) * SC, hi2f(u1) * SC);
          sc[i] = lo2f(t0); sc[i + 1] = hi2f(t0); sc[4 + i] = lo2f(t1); sc[4 + i + 1] = hi2f(t1);
        }
        const float bm = xg_max(fmaxf(fmaxf(fmaxf(sc[0], sc[1]), fmaxf(sc[2], sc[3])), fmaxf(fmaxf(sc[4], sc[5]), fmaxf(sc[6], sc[7]))));
        const float mn = fmaxf(m, bm);
        const float alpha = __builtin_amdgcn_exp2f((m - mn) * L2E);
        m = mn; acc0 *= alpha; acc1 *= alpha;
        const float mb = mn * L2E;
        float pr[8], ps = 0.f;
#pragma unroll
        for (int i = 0; i < 8; ++i) { pr[i] = __builtin_amdgcn_exp2f(sc[i] * L2E - mb); ps += pr[i]; }
        l = l * alpha + ps;
        v4u pf = {cvtpk(pr[0], pr[1]), cvtpk(pr[2], pr[3]), cvtpk(pr[4], pr[5]), cvtpk(pr[6], pr[7])};
        asm volatile("" : "+v"(pf), "+v"(acc0), "+v"(acc1), "+v"(l));
        for (unsigned spins = 0; full < (unsigned)(2 * u + 2);) { full = __builtin_amdgcn_readfirstlane(*FULL); if (++spins > SPIN_L) full = SENT; }
        asm volatile("" ::: "memory");
        const v2u a00 = __builtin_bit_cast(v2u, __builtin_amdgcn_ds_read_tr16_b64_v4i16((LAS s16x4*)(S + va0)));
        const v2u a01 = __builtin_bit_cast(v2u, __builtin_amdgcn_ds_read_tr16_b64_v4i16((LAS s16x4*)(S + va0 + 4096)));
        const v2u a10 = __builtin_bit_cast(v2u, __builtin_amdgcn_ds_read_tr16_b64_v4i16((LAS s16x4*)(S + va1)));
        const v2u a11 = __builtin_bit_cast(v2u, __builtin_amdgcn_ds_read_tr16_b64_v4i16((LAS s16x4*)(S + va1 + 4096)));
        asm volatile("s_waitcnt lgkmcnt(0)" ::: "memory");
        *(volatile LAS unsigned*)(L + L_FREE + c * 4) = (unsigned)(u + 1);
        acc0 = mfma(v4u{a00[0], a00[1], a01[0], a01[1]}, pf, acc0);
        acc1 = mfma(v4u{a10[0], a10[1], a11[0], a11[1]}, pf, acc1);
        slot = (slot + 1 == NSLOT) ? 0 : slot + 1;
      }
    }
    const rsrc_t rt = mkrs(p.ws, pass ? W_END : 0);
    if (w >= 4 || !pass) {
      l = xg_sum(l);
      f32x4 v = n < 4 ? acc0 : acc1;
      if (n >= 8) v = f32x4{m, l, m, l};
      int o = W_PO + bid * 2048 + (n & 3) * 512 + (w & 3) * 128 + (n >> 2) * 64 + g * 16;
      if (n >= 8) o = (n < 12 && g == 0 && w == 4) ? W_ML + bid * 128 + (n & 3) * 16 : (int)OOB;
      const v4u vu = __builtin_bit_cast(v4u, v);
      ST128((v4u{nosent(vu[0]), nosent(vu[1]), nosent(vu[2]), nosent(vu[3])}), rt, o, 0, AUX_SC1);
    }
    if (pass) { STAMP(5); bar(); }
    const v2u ga = *(LAS v2u*)(L + L_MOD + w * 128 + n * 8), sb = *(LAS v2u*)(L + L_MOD + 1024 + w * 128 + n * 8), cb = *(LAS v2u*)(L + L_MOD + 2048 + w * 128 + n * 8);
    const v2u gb = *(LAS v2u*)(L + L_MOD + 3072 + ocol * 2);
    {
      v4u o4[4]; v2u ml[4];
      const int po = W_PO + (bid & ~15) * 2048 + w * 256, mo = W_ML + (bid & ~15) * 128 + (w >> 1) * 16;
      for (unsigned spins = 0;;) {
        unsigned mx = 0;
#pragma unroll
        for (int i = 0; i < 4; ++i) { o4[i] = LD128(rt, (4 * i + g) * 2048 + n * 16, po, AUX_SC1); ml[i] = LD64(rt, (4 * i + g) * 128, mo, AUX_SC1); }
#pragma unroll
        for (int i = 0; i < 4; ++i) mx = max(max(mx, umax4(o4[i])), max(ml[i][0], ml[i][1]));
        if (__all(mx != SENT) || ++spins > SPIN_G) break;
        if (POLL_SLEEP) __builtin_amdgcn_s_sleep(POLL_SLEEP);
        asm volatile("" ::: "memory");
      }
      if (pass) STAMP(6);
      float mi[4], M;
#pragma unroll
      for (int i = 0; i < 4; ++i) mi[i] = __uint_as_float(ml[i][0]);
      M = xg_max(fmaxf(fmaxf(mi[0], mi[1]), fmaxf(mi[2], mi[3])));
      float Ls = 0.f; f32x4 o = {0.f, 0.f, 0.f, 0.f};
#pragma unroll
      for (int i = 0; i < 4; ++i) { const float e = __builtin_amdgcn_exp2f((mi[i] - M) * L2E); Ls += e * __uint_as_float(ml[i][1]); o += e * __builtin_bit_cast(f32x4, o4[i]); }
      const float inv = rcp(xg_sum(Ls));
      const unsigned p0 = cvtpk(xg_sum(o[0]) * inv, xg_sum(o[1]) * inv), p1 = cvtpk(xg_sum(o[2]) * inv, xg_sum(o[3]) * inv);
      float y[8];
#pragma unroll
      for (int k = 0; k < 8; ++k) y[k] = row_sum(dot2(p1, wo_p[8 + k], dot2(p0, wo_p[k], 0.f)));
      if (n < 2) *(LAS f32x4*)(L + L_RAB + w * 128 + g * 32 + n * 16) = n ? f32x4{y[4], y[5], y[6], y[7]} : f32x4{y[0], y[1], y[2], y[3]};
      if (pass) bar();
      if (w == 3) {
        f32x4 s = *(LAS f32x4*)(L + L_RAB + (lane >> 3) * 128 + (lane & 7) * 16);
#pragma unroll
        for (int k = 0; k < 4; ++k) { float t = s[k]; t += dpp<0x128>(t); s[k] = xg_sum(t); }
        const v4u su = __builtin_bit_cast(v4u, s);
        ST128((v4u{nosent(su[0]), nosent(su[1]), nosent(su[2]), nosent(su[3])}), rt, lane < 8 ? lane * 16 : (int)OOB, W_AB + ((b * 8 + kvh) * 512 + cblk * 32) * 4, AUX_SC1);
        if (pass) STAMP(7);
      }
    }
    {
      v4u a[2];
      for (unsigned spins = 0;;) {
#pragma unroll
        for (int i = 0; i < 2; ++i) a[i] = LD128(rt, (4 * i + g) * 2048 + n * 16, W_AB + b * 16384 + w * 256, AUX_SC1);
        if (__all(max(umax4(a[0]), umax4(a[1])) != SENT) || ++spins > SPIN_G) break;
        if (POLL_SLEEP) __builtin_amdgcn_s_sleep(POLL_SLEEP);
        asm volatile("" ::: "memory");
      }
      if (pass) STAMP(8);
      const f32x4 as = __builtin_bit_cast(f32x4, a[0]) + __builtin_bit_cast(f32x4, a[1]);
      float x1[4];
      x1[0] = rbf(lo2f(xin[0]) + rbf(lo2f(ga[0]) * rbf(xg_sum(as[0])))); x1[1] = rbf(hi2f(xin[0]) + rbf(hi2f(ga[0]) * rbf(xg_sum(as[1]))));
      x1[2] = rbf(lo2f(xin[1]) + rbf(lo2f(ga[1]) * rbf(xg_sum(as[2])))); x1[3] = rbf(hi2f(xin[1]) + rbf(hi2f(ga[1]) * rbf(xg_sum(as[3]))));
      const float mw = row_sum((x1[0] + x1[1]) + (x1[2] + x1[3])) * (1.f / 64.f);
      float q = 0.f;
#pragma unroll
      for (int k = 0; k < 4; ++k) { const float t = x1[k] - mw; q += t * t; }
      q = row_sum(q);
      if (lane == 0) *(LAS f32x2*)(L + L_ST + w * 8) = f32x2{mw, q};
      if (w == xl && lane == j16) *(LAS f32x4*)(L + L_X1) = f32x4{x1[0], x1[1], x1[2], x1[3]};
      if (pass) bar();
      f32x4 st[4];
#pragma unroll
      for (int k = 0; k < 4; ++k) st[k] = *(LAS f32x4*)(L + L_ST + k * 16);
      const float mu = (((st[0][0] + st[0][2]) + (st[1][0] + st[1][2])) + ((st[2][0] + st[2][2]) + (st[3][0] + st[3][2]))) * 0.125f;
      float m2 = 0.f, dv = 0.f;
#pragma unroll
      for (int k = 0; k < 4; ++k) { m2 += st[k][1] + st[k][3]; const float t0 = st[k][0] - mu, t1 = st[k][2] - mu; dv += t0 * t0 + t1 * t1; }
      const float mean = rbf(mu), var = rbf((m2 + 64.f * dv) * (1.f / 512.f));
      const float isd = rcp(rbf(__builtin_amdgcn_sqrtf(rbf(var + 1e-5f))));
      const float h0 = rbf(rbf(rbf(x1[0] - mean) * isd) * rbf(1.f + lo2f(cb[0]))) + lo2f(sb[0]), h1 = rbf(rbf(rbf(x1[1] - mean) * isd) * rbf(1.f + hi2f(cb[0]))) + hi2f(sb[0]);
      const float h2 = rbf(rbf(rbf(x1[2] - mean) * isd) * rbf(1.f + lo2f(cb[1]))) + lo2f(sb[1]), h3 = rbf(rbf(rbf(x1[3] - mean) * isd) * rbf(1.f + hi2f(cb[1]))) + hi2f(sb[1]);
      const unsigned p0 = cvtpk(h0, h1), p1 = cvtpk(h2, h3);
      f32x4 y;
#pragma unroll
      for (int k = 0; k < 4; ++k) y[k] = row_sum(dot2(p1, w1_p[4 + k], dot2(p0, w1_p[k], 0.f)));
      if (n == 0) *(LAS f32x4*)(L + L_RC + w * 64 + g * 16) = y;
      if (pass) bar();
      if (w == 3) {
        const f32x2 s = *(LAS f32x2*)(L + L_RC + (lane >> 3) * 64 + (lane & 7) * 8);
        float t0 = s[0], t1 = s[1];
        t0 += dpp<0x128>(t0); t1 += dpp<0x128>(t1);
        t0 = xg_sum(t0); t1 = xg_sum(t1);
        const unsigned d = nosent(cvtpk(silu_f(rbf(t0)), silu_f(rbf(t1))));
        const int di = (int)d;
        const v4u o = {d, (unsigned)__builtin_amdgcn_mov_dpp(di, 0x55, 0xf, 0xf, true), (unsigned)__builtin_amdgcn_mov_dpp(di, 0xAA, 0xf, 0xf, true), (unsigned)__builtin_amdgcn_mov_dpp(di, 0xFF, 0xf, 0xf, true)};
        ST128(o, rt, (lane == 0 || lane == 4) && hasC ? lane * 4 : (int)OOB, W_H1 + (b * 112 + fblk) * 128, AUX_SC1);
        if (pass) STAMP(9);
      }
    }
    {
      v2u hv;
      const int e = w * 56 + lane;
      for (unsigned spins = 0;;) {
        hv = LD64(rt, lane < 56 ? (e >> 2) * 128 + (e & 3) * 8 : (int)OOB, W_H1 + b * 112 * 128, AUX_SC1);
        if (__all(max(hv[0], hv[1]) != SENT) || ++spins > SPIN_G) break;
        if (POLL_SLEEP) __builtin_amdgcn_s_sleep(POLL_SLEEP);
        asm volatile("" ::: "memory");
      }
      if (pass) STAMP(10);
      float y[4];
#pragma unroll
      for (int k = 0; k < 4; ++k) y[k] = dot2(hv[1], w2_p[4 + k], dot2(hv[0], w2_p[k], 0.f));
      const float t = row_sum(tr16(tr32(y[0], y[2]), tr32(y[1], y[3])));
      if (n == 0) *(LAS float*)(L + L_RD + g * 32 + w * 4) = t;
      if (pass) bar();
      if (w == 3) {
        const f32x4 r0 = *(LAS f32x4*)(L + L_RD + (lane & 3) * 32), r1 = *(LAS f32x4*)(L + L_RD + (lane & 3) * 32 + 16);
        const float y2 = ((r0[0] + r0[1]) + (r0[2] + r0[3])) + ((r1[0] + r1[1]) + (r1[2] + r1[3]));
        const float xv = *(LAS float*)(L + L_X1 + (lane & 3) * 4);
        const unsigned gw = (lane & 2) ? gb[1] : gb[0];
        const float gv = (lane & 1) ? hi2f(gw) : lo2f(gw);
        const float r = xv + rbf(gv * rbf(y2));
        const v2u o = {cvtpk(dpp<0x00>(r), dpp<0x55>(r)), cvtpk(dpp<0xAA>(r), dpp<0xFF>(r))};
        if (pass && lane == 0) *(GAS v2u*)(p.out + b * 512 + ocol) = o;
        __builtin_amdgcn_raw_buffer_store_b32(tag, rt, (lane == 0 && bid == 0) ? 0 : (int)OOB, W_EPOCH, AUX_SC1);
        if (pass) STAMP(11);
      }
    }
    if (pass) break;
    passv = 1;
  }
}

extern "C" void kernel_launch(void* const* d_in, const int* in_sizes, int n_in, void* d_out, int out_size, void* d_ws, size_t ws_size, hipStream_t stream) {
  Params p;
  p.x = (const u16*)d_in[0]; p.K = (const u16*)d_in[1]; p.V = (const u16*)d_in[2]; p.wq = (const u16*)d_in[3]; p.wo = (const u16*)d_in[4];
  p.w1 = (const u16*)d_in[6]; p.w2 = (const u16*)d_in[7]; p.c = (const u16*)d_in[8]; p.wmod = (const u16*)d_in[9];
  p.out = (u16*)d_out; p.ws = (unsigned char*)d_ws;
  (void)hipFuncSetAttribute((const void*)adaln_block_kernel, hipFuncAttributeMaxDynamicSharedMemorySize, L_END);
  hipLaunchKernelGGL(adaln_block_kernel, dim3(NWG), dim3(NTHR), L_END, stream, p);
}
```

```cpp
#include <hip/hip_runtime.h>
#include <hip/hip_bf16.h>
#include <stdint.h>

typedef unsigned short u16;
typedef unsigned long long u64;
typedef unsigned v4u __attribute__((ext_vector_type(4)));
typedef unsigned v2u __attribute__((ext_vector_type(2)));
typedef float f32x4 __attribute__((ext_vector_type(4)));
typedef float f32x2 __attribute__((ext_vector_type(2)));
typedef short bf16x8 __attribute__((ext_vector_type(8)));
typedef short s16x4 __attribute__((ext_vector_type(4)));
typedef __bf16 bf16x2_t __attribute__((ext_vector_type(2)));
typedef __amdgpu_buffer_rsrc_t rsrc_t;
#define GAS __attribute__((address_space(1)))
#define LAS __attribute__((address_space(3)))
#define RLX_AGENT __ATOMIC_RELAXED, __HIP_MEMORY_SCOPE_AGENT

#define NSLOT 9
#define DEPTH 24
#define DRYRUN 1
#define FH 2
#define NSLOW 0
#define DSLOW 16
#define DDEEP 32
#define QHOLD 1
#define QCATCH 24
#define CODEWARM 1
#define AUX_W 0
#define ABL 0
#define WSP 8
#define WORD 1
#define WBASE 20
#define LPRIO 2
#define POLL_SLEEP 0
#define HSTAG 40
#define QTHR 9
#define QSTAG 4

constexpr int NWG = 256, NWAVE = 13, NTHR = NWAVE * 64, NUNIT = 64;
constexpr int NH = 16 * FH, Q0 = NH, M0 = Q0 + 64, JEND = M0 + 32;
constexpr unsigned SENT = 0xFFFFFFFFu, OOB = 0x7FFFFFF0u;
constexpr int AUX_NT = 2, AUX_SC1 = 16;
constexpr int W_EPOCH = 0, W_CL = 0x800, W_QG = 0x2000  , W_MG = 0xA000  , W_AB = 0x10000, W_ML = 0x18000,
              W_PO = 0x20000, W_OP = 0xA0000  , W_HG = 0x110000  , W_END = 0x120000;
constexpr int W_STAMP = 0x200000;
constexpr int L_CTRL = NSLOT * 16384, L_FULL = L_CTRL, L_QRDY = L_CTRL + 4, L_JCNT = L_CTRL + 8, L_ECNT = L_CTRL + 12, L_FREE = L_CTRL + 16, L_Q = L_CTRL + 64,
              L_RED = L_Q + 1024  , L_HX = L_RED + 4096  , L_RAB = L_HX + 2048, L_ST = L_RAB + 1024, L_RC = L_ST + 64, L_X1 = L_RC + 512,
              L_STAMP = L_X1 + 16, L_END = L_STAMP + NWAVE * 128;
static_assert(L_END <= 160 * 1024, "LDS");
constexpr unsigned SPIN_G = 1u << 17, SPIN_L = 1u << 22;

#define STAMP(k) do {} while (0)
#define TSTAMP(k) do {} while (0)
#define PSTAMP(k) do {} while (0)
#define STAMP_INIT() do {} while (0)
#define STAMP_OUT() do {} while (0)

__device__ __forceinline__ float lo2f(unsigned u) { return __builtin_bit_cast(float, u << 16); }
__device__ __forceinline__ float hi2f(unsigned u) { return __builtin_bit_cast(float, u & 0xffff0000u); }
__device__ __forceinline__ unsigned cvtpk(float lo, float hi) { f32x2 v = {lo, hi}; return __builtin_bit_cast(unsigned, __builtin_convertvector(v, bf16x2_t)); }
__device__ __forceinline__ float rbf(float x) { return lo2f(cvtpk(x, x)); }
__device__ __forceinline__ float rcp(float x) { return __builtin_amdgcn_rcpf(x); }
__device__ __forceinline__ float silu_f(float x) { return x * rcp(1.f + __builtin_amdgcn_exp2f(x * -1.4426950408889634f)); }
__device__ __forceinline__ f32x4 mfma(v4u a, v4u b, f32x4 c) {
  return __builtin_amdgcn_mfma_f32_16x16x32_bf16(__builtin_bit_cast(bf16x8, a), __builtin_bit_cast(bf16x8, b), c, 0, 0, 0);
}
__device__ __forceinline__ float dot2(unsigned a, unsigned b, float c) {
  return __builtin_amdgcn_fdot2_f32_bf16(__builtin_bit_cast(bf16x2_t, a), __builtin_bit_cast(bf16x2_t, b), c, false);
}
__device__ __forceinline__ unsigned pk_lo(unsigned r0, unsigned r1) { return __builtin_amdgcn_perm(r1, r0, 0x05040100u); }
__device__ __forceinline__ unsigned pk_hi(unsigned r0, unsigned r1) { return __builtin_amdgcn_perm(r1, r0, 0x07060302u); }
__device__ __forceinline__ unsigned nosent(unsigned d) { return d < 0xFFFFFFFEu ? d : 0xFFFFFFFEu; }
template <int CTRL> __device__ __forceinline__ float dpp(float x) {
  return __builtin_bit_cast(float, __builtin_amdgcn_mov_dpp(__builtin_bit_cast(int, x), CTRL, 0xf, 0xf, true));
}
__device__ __forceinline__ float row_sum(float x) { x += dpp<0x128>(x); x += dpp<0x124>(x); x += dpp<0x122>(x); x += dpp<0x121>(x); return x; }
__device__ __forceinline__ float tr32(float a, float b) {
  auto s = __builtin_amdgcn_permlane32_swap(__float_as_uint(a), __float_as_uint(b), false, false);
  return __uint_as_float(s[0]) + __uint_as_float(s[1]);
}
__device__ __forceinline__ float tr16(float a, float b) {
  auto s = __builtin_amdgcn_permlane16_swap(__float_as_uint(a), __float_as_uint(b), false, false);
  return __uint_as_float(s[0]) + __uint_as_float(s[1]);
}
__device__ __forceinline__ float xg_sum(float x) { x = tr16(x, x); return tr32(x, x); }
__device__ __forceinline__ float xg_max(float x) {
  auto s = __builtin_amdgcn_permlane16_swap(__float_as_uint(x), __float_as_uint(x), false, false);
  x = fmaxf(__uint_as_float(s[0]), __uint_as_float(s[1]));
  auto t = __builtin_amdgcn_permlane32_swap(__float_as_uint(x), __float_as_uint(x), false, false);
  return fmaxf(__uint_as_float(t[0]), __uint_as_float(t[1]));
}
__device__ __forceinline__ float wave_sum(float x) { return xg_sum(row_sum(x)); }
__device__ __forceinline__ unsigned umax4(v4u v) { return max(max(v[0], v[1]), max(v[2], v[3])); }
template <int N> __device__ __forceinline__ void waitvm() { asm volatile("s_waitcnt vmcnt(%0)" :: "n"(N) : "memory"); }
__device__ __forceinline__ void bar() { asm volatile("s_waitcnt lgkmcnt(0)\n\ts_barrier" ::: "memory"); }
__device__ __forceinline__ rsrc_t mkrs(const void* p, unsigned bytes) { return __builtin_amdgcn_make_buffer_rsrc((void*)p, 0, bytes, 0x00020000); }
__device__ __forceinline__ void dma4(rsrc_t rs, int v0, int v1, int v2, int v3, int soff, unsigned lds) {
  asm volatile("s_mov_b32 m0, %6\n\ts_nop 0\n\tbuffer_load_dwordx4 %0, %4, %5 offen nt lds\n\tbuffer_load_dwordx4 %1, %4, %5 offen offset:1024 nt lds\n\t"
               "buffer_load_dwordx4 %2, %4, %5 offen offset:2048 nt lds\n\tbuffer_load_dwordx4 %3, %4, %5 offen offset:3072 nt lds"
               :: "v"(v0), "v"(v1), "v"(v2), "v"(v3), "s"(rs), "s"(soff), "s"(lds) : "memory");
}
#define LD128(rs, vo, so, aux) __builtin_amdgcn_raw_buffer_load_b128(rs, vo, so, aux)
#define LD64(rs, vo, so, aux) __builtin_amdgcn_raw_buffer_load_b64(rs, vo, so, aux)
#define LD32(rs, vo, so, aux) __builtin_amdgcn_raw_buffer_load_b32(rs, vo, so, aux)
#define ST128(v, rs, vo, so, aux) __builtin_amdgcn_raw_buffer_store_b128(v, rs, vo, so, aux)
#define ST64(v, rs, vo, so, aux) __builtin_amdgcn_raw_buffer_store_b64(v, rs, vo, so, aux)

struct Params { const u16 *x, *K, *V, *wq, *wo, *w1, *w2, *c, *wmod; u16* out; unsigned char* ws; };

__device__ __forceinline__ unsigned lds_get(LAS unsigned char* L, int off) { return __builtin_amdgcn_readfirstlane(*(volatile LAS unsigned*)(L + off)); }
template <int SL> __device__ __forceinline__ void lds_wait(LAS unsigned char* L, int off, unsigned want) {
#pragma unroll 1
  for (unsigned spins = 0; lds_get(L, off) < want && ++spins < SPIN_L;) __builtin_amdgcn_s_sleep(SL);
  asm volatile("" ::: "memory");
}

__global__ void __launch_bounds__(NTHR, 4) __attribute__((aligned(4096))) adaln_block_kernel(Params p) {
  extern __shared__ __attribute__((aligned(16))) unsigned char smem[];
  LAS unsigned char* L = (LAS unsigned char*)smem;
  const int tid = threadIdx.x, lane = tid & 63, w = __builtin_amdgcn_readfirstlane(tid >> 6);
  const int bid = blockIdx.x;
  unsigned cw[5] = {0u, 0u, 0u, 0u, 0u};
  if (CODEWARM && w == 9 && (CODEWARM == 2 || (bid >> 4) == 8)) {
    u64 pc0; unsigned clen;
    asm volatile("s_getpc_b64 %0\n.Ladaln_code_begin:\n\ts_mov_b32 %1, .Ladaln_code_end-.Ladaln_code_begin" : "=s"(pc0), "=s"(clen));
    const rsrc_t rc = mkrs((const void*)pc0, clen);
#pragma unroll
    for (int i = 0; i < 5; ++i) cw[i] = LD32(rc, lane * 64, i * 4096, 0);
  }
  STAMP_INIT();
  if (tid < 16) ((LAS unsigned*)(L + L_CTRL))[tid] = 0u;
  asm volatile("s_waitcnt lgkmcnt(0)\n\ts_barrier" : "+s"(p.x), "+s"(p.K), "+s"(p.V), "+s"(p.wq), "+s"(p.wo), "+s"(p.w1), "+s"(p.w2), "+s"(p.c), "+s"(p.wmod), "+s"(p.out), "+s"(p.ws) :: "memory");
  const int xl = bid & 7, jl = bid >> 3, b = bid >> 7, kvh = (bid >> 4) & 7, mem = bid & 15;
  const int g = lane >> 4, n = lane & 15, r8 = lane >> 3, c8 = lane & 7;
  volatile LAS unsigned* FULL = (volatile LAS unsigned*)(L + L_FULL);
  constexpr float L2E = 1.4426950408889634f;
  const bool isH = bid < Q0, isQ = !isH && bid < M0;
  const rsrc_t rw = mkrs(p.ws, W_END);
  const int cblk = 2 * ((mem + kvh) & 7) + (mem >> 3);
  const int j16 = jl & 15;
  const bool hasC = j16 < 14;
  const int fblk = xl * 14 + j16;
  const int ocol = xl * 64 + j16 * 4;

  if (w >= 8 && w < 12) {
  const int c = w - 8;
  asm volatile("" :: "v"(cw[0]), "v"(cw[1]), "v"(cw[2]), "v"(cw[3]), "v"(cw[4]));
  if (c == 0) {
    unsigned ep = LD32(rw, 0, W_EPOCH, AUX_SC1);
    lds_wait<4>(L, L_FULL, 16);
    asm volatile("" : "+v"(ep));
    const unsigned tag = ep + 1u;
    const v4u s4 = {SENT, SENT, SENT, SENT};
    ST128(s4, rw, lane * 16, W_PO + bid * 2048, AUX_SC1);
    ST128(s4, rw, lane * 16 + 1024, W_PO + bid * 2048, AUX_SC1);
    const int oo = hasC ? W_OP + (b * 16 * 112 + fblk) * 128 : (int)OOB;
    ST128(s4, rw, r8 * (112 * 128) + c8 * 16, oo, AUX_SC1);
    ST128(s4, rw, (8 + r8) * (112 * 128) + c8 * 16, oo, AUX_SC1);
    ST128(s4, rw, lane < 4 ? W_ML + bid * 128 + lane * 16 : lane < 12 ? W_AB + ((b * 8 + kvh) * 512 + cblk * 32) * 4 + (lane - 4) * 16 : (int)OOB, 0, AUX_SC1);
    waitvm<0>();
    lds_wait<2>(L, L_ECNT, 8);
    if (lane == 0) __hip_atomic_store((GAS u64*)(p.ws + W_CL + bid * 8), ((u64)tag << 32) | (tag ^ 0x5bd1e995u), RLX_AGENT);
    STAMP(2);
  }
  f32x4 acc0 = {0.f, 0.f, 0.f, 0.f}, acc1 = {0.f, 0.f, 0.f, 0.f};
  float m = -INFINITY, l = 0.f;
  lds_wait<1>(L, L_QRDY, 1);
  STAMP(1);
  v4u qf[4];
#pragma unroll
  for (int s = 0; s < 4; ++s) qf[s] = *(LAS v4u*)(L + L_Q + (n & 3) * 256 + s * 64 + g * 16);
  const int r7 = 4 * (g & 1) + (n >> 2);
  const int va0 = 8192 + (4 * g + (n >> 2)) * 256 + (((4 * c + ((n >> 1) & 1)) ^ (2 * r7)) * 16) + 8 * (n & 1), va1 = va0 ^ 32;
  int po = W_PO + bid * 2048 + (n & 3) * 512 + c * 128 + (n >> 2) * 64 + g * 16;
  if (n >= 8) po = (n < 12 && g == 0 && c == 0) ? W_ML + bid * 128 + (n & 3) * 16 : (int)OOB;
  asm volatile("" : "+v"(po));
  const int ka = n * 256 + ((g ^ n) * 16);
  unsigned full = 0; int slot = 0;
  float lt = 0.f, mb = 0.f;
  constexpr float SC = 0.08838834764831845f;
#pragma unroll 1
  for (int u = 0; u < NUNIT; ++u) {
    LAS unsigned char* S = L + slot * 16384;
    for (unsigned spins = 0; full < (unsigned)(16 * u + 8);) { full = __builtin_amdgcn_readfirstlane(*FULL); if (++spins > SPIN_L) full = SENT; }
    asm volatile("" ::: "memory");
    v4u kf[8];
#pragma unroll
    for (int i = 0; i < 8; ++i) kf[i] = *(LAS v4u*)(S + (ka ^ ((i & 3) * 64)) + (i >> 2) * 4096);
    __builtin_amdgcn_sched_barrier(0);
    f32x4 s0 = {0.f, 0.f, 0.f, 0.f}, s1 = {0.f, 0.f, 0.f, 0.f};
#pragma unroll
    for (int s = 0; s < 4; ++s) { s0 = mfma(kf[s], qf[s], s0); s1 = mfma(kf[4 + s], qf[s], s1); }
    const unsigned u0 = cvtpk(s0[0], s0[1]), u1 = cvtpk(s0[2], s0[3]), u2 = cvtpk(s1[0], s1[1]), u3 = cvtpk(s1[2], s1[3]);
    const unsigned us = (n & 8) ? ((n & 4) ? u3 : u2) : ((n & 4) ? u1 : u0);
    const unsigned ts = cvtpk(lo2f(us) * SC, hi2f(us) * SC);
    const float e0 = lo2f(ts), e1 = hi2f(ts);
    float bm = fmaxf(e0, e1);
    if (__any(bm > m)) {
      bm = fmaxf(bm, dpp<0x124>(bm)); bm = fmaxf(bm, dpp<0x128>(bm));
      const float mn = fmaxf(m, xg_max(bm));
      const float alpha = __builtin_amdgcn_exp2f((m - mn) * L2E);
      m = mn; mb = mn * L2E; acc0 *= alpha; acc1 *= alpha; l *= alpha;
    }
    const float p0 = __builtin_amdgcn_exp2f(e0 * L2E - mb), p1 = __builtin_amdgcn_exp2f(e1 * L2E - mb);
    l += p0 + p1;
    const int pp = (int)cvtpk(p0, p1);
    v4u pf = {(unsigned)pp, (unsigned)__builtin_amdgcn_mov_dpp(pp, 0x12C, 0xf, 0xf, true), (unsigned)__builtin_amdgcn_mov_dpp(pp, 0x128, 0xf, 0xf, true), (unsigned)__builtin_amdgcn_mov_dpp(pp, 0x124, 0xf, 0xf, true)};
    if (u == NUNIT - 1) { lt = l + dpp<0x124>(l); lt += dpp<0x128>(lt); lt = xg_sum(lt); STAMP(3); }
    asm volatile("" : "+v"(pf), "+v"(acc0), "+v"(acc1), "+v"(l), "+v"(lt));
    for (unsigned spins = 0; full < (unsigned)(16 * u + 16);) { full = __builtin_amdgcn_readfirstlane(*FULL); if (++spins > SPIN_L) full = SENT; }
    asm volatile("" ::: "memory");
    const v2u a00 = __builtin_bit_cast(v2u, __builtin_amdgcn_ds_read_tr16_b64_v4i16((LAS s16x4*)(S + va0)));
    const v2u a01 = __builtin_bit_cast(v2u, __builtin_amdgcn_ds_read_tr16_b64_v4i16((LAS s16x4*)(S + va0 + 4096)));
    const v2u a10 = __builtin_bit_cast(v2u, __builtin_amdgcn_ds_read_tr16_b64_v4i16((LAS s16x4*)(S + va1)));
    const v2u a11 = __builtin_bit_cast(v2u, __builtin_amdgcn_ds_read_tr16_b64_v4i16((LAS s16x4*)(S + va1 + 4096)));
    asm volatile("s_waitcnt lgkmcnt(0)" ::: "memory");
    *(volatile LAS unsigned*)(L + L_FREE + c * 4) = (unsigned)(u + 1);
    acc0 = mfma(v4u{a00[0], a00[1], a01[0], a01[1]}, pf, acc0);
    acc1 = mfma(v4u{a10[0], a10[1], a11[0], a11[1]}, pf, acc1);
    slot = (slot + 1 == NSLOT) ? 0 : slot + 1;
  }
  {
    f32x4 v = acc0;
#pragma unroll
    for (int k = 0; k < 4; ++k) { const float t = dpp<0x124>(acc1[k]); v[k] = n < 4 ? v[k] : t; }
    if (n >= 8) v = f32x4{m, lt, m, lt};
    const v4u vu = __builtin_bit_cast(v4u, v);
    ST128((v4u{nosent(vu[0]), nosent(vu[1]), nosent(vu[2]), nosent(vu[3])}), rw, po, 0, AUX_SC1);
  }
  STAMP(5);
  bar();
  asm volatile(".Ladaln_code_end:");
  STAMP_OUT();
  return;
  }

  if (w < 8) {
    unsigned ep = LD32(rw, 0, W_EPOCH, AUX_SC1);
    const int nact = isH ? 8 / FH : 8;
    if (bid < JEND && w < nact) {
      const int hl = bid & 15, hf = bid >> 4;
      const int pitch = isQ ? 8192 : 6144;
      const int cb = (isH ? hl : isQ ? bid - Q0 : bid - M0 + 16) * 128;
      const int row0 = (isH ? hf * (512 / FH) : 0) + w * 64;
      const rsrc_t rj = mkrs(isQ ? p.wq : p.wmod, 512 * pitch), ri = mkrs(isQ ? p.x : p.c, 2048);
      const int io = isQ ? lane * 16 : row0 * 2 + r8 * 16;
      const v4u i0 = LD128(ri, io, 0, 0), i1 = LD128(ri, io, 1024, 0);
      unsigned xd0 = __builtin_amdgcn_raw_buffer_load_b16(ri, lane * 2, w * 128, 0), xd1 = __builtin_amdgcn_raw_buffer_load_b16(ri, lane * 2, 1024 + w * 128, 0);
      v4u wr[8];
#pragma unroll
      for (int i = 0; i < 8; ++i) wr[i] = LD128(rj, r8 * 8 * pitch + c8 * 16, (row0 + i) * pitch + cb, AUX_NT);
      __builtin_amdgcn_sched_barrier(0);
      STAMP(9);
      v4u a0, a1;
      float ln0 = 0.f, ln1 = 0.f;
      if (!isQ) {
#pragma unroll
        for (int k = 0; k < 4; ++k) { a0[k] = cvtpk(silu_f(lo2f(i0[k])), silu_f(hi2f(i0[k]))); a1[k] = cvtpk(silu_f(lo2f(i1[k])), silu_f(hi2f(i1[k]))); }
      } else {
        a0 = i0; a1 = i1;
        asm volatile("" : "+v"(xd0), "+v"(xd1));
        float sm0 = 0.f, sm1 = 0.f;
#pragma unroll
        for (int k = 0; k < 4; ++k) { sm0 += lo2f(i0[k]) + hi2f(i0[k]); sm1 += lo2f(i1[k]) + hi2f(i1[k]); }
        const float mean0 = wave_sum(sm0) * (1.f / 512.f), mean1 = wave_sum(sm1) * (1.f / 512.f);
        float q0 = 0.f, q1 = 0.f;
#pragma unroll
        for (int k = 0; k < 4; ++k) {
          float t = lo2f(i0[k]) - mean0; q0 += t * t; t = hi2f(i0[k]) - mean0; q0 += t * t;
          t = lo2f(i1[k]) - mean1; q1 += t * t; t = hi2f(i1[k]) - mean1; q1 += t * t;
        }
        const float isd0 = rcp(rbf(__builtin_amdgcn_sqrtf(rbf(rbf(wave_sum(q0) * (1.f / 512.f)) + 1e-5f))));
        const float isd1 = rcp(rbf(__builtin_amdgcn_sqrtf(rbf(rbf(wave_sum(q1) * (1.f / 512.f)) + 1e-5f))));
        ln0 = rbf(rbf(lo2f(xd0) - rbf(mean0)) * isd0); ln1 = rbf(rbf(lo2f(xd1) - rbf(mean1)) * isd1);
      }
      asm volatile("" : "+v"(a0), "+v"(a1), "+v"(ln0), "+v"(ln1));
      unsigned wp[32];
#pragma unroll
      for (int pp = 0; pp < 4; ++pp)
#pragma unroll
        for (int d = 0; d < 4; ++d) { wp[pp * 8 + 2 * d] = pk_lo(wr[2 * pp][d], wr[2 * pp + 1][d]); wp[pp * 8 + 2 * d + 1] = pk_hi(wr[2 * pp][d], wr[2 * pp + 1][d]); }
#pragma unroll
      for (int k = 0; k < 32; k += 8) asm volatile("" : "+v"(wp[k]), "+v"(wp[k + 1]), "+v"(wp[k + 2]), "+v"(wp[k + 3]), "+v"(wp[k + 4]), "+v"(wp[k + 5]), "+v"(wp[k + 6]), "+v"(wp[k + 7]));
      STAMP(1);
      if (isQ) {
        asm volatile("" : "+v"(ep));
        const unsigned tag = ep + 1u;
        v4u t[2 * FH];
        if (HSTAG) __builtin_amdgcn_s_sleep(HSTAG);
#pragma unroll 1
        for (unsigned spins = 0;;) {
          bool ok = true;
          PSTAMP(11);
#pragma unroll
          for (int k = 0; k < 2 * FH; ++k) t[k] = LD128(rw, lane * 16 + k * 1024, W_HG + w * (2048 * FH), AUX_SC1);
#pragma unroll
          for (int k = 0; k < 2 * FH; ++k) ok &= (t[k][1] == tag) & (t[k][3] == tag);
          if (__all(ok) || ++spins > SPIN_G) break;
          asm volatile("" ::: "memory");
        }
        STAMP(2);
        float sa0 = 0.f, sa1 = 0.f, ca0 = 0.f, ca1 = 0.f;
#pragma unroll
        for (int k = 0; k < FH; ++k) { sa0 += __uint_as_float(t[k][0]); sa1 += __uint_as_float(t[k][2]); ca0 += __uint_as_float(t[FH + k][0]); ca1 += __uint_as_float(t[FH + k][2]); }
        const unsigned hh = cvtpk(rbf(ln0 * rbf(1.f + rbf(ca0))) + rbf(sa0), rbf(ln1 * rbf(1.f + rbf(ca1))) + rbf(sa1));
        *(LAS u16*)(L + L_HX + w * 256 + lane * 2) = (u16)hh; *(LAS u16*)(L + L_HX + w * 256 + 128 + lane * 2) = (u16)(hh >> 16);
        asm volatile("" ::: "memory");
        a0 = *(LAS v4u*)(L + L_HX + w * 256 + r8 * 16); a1 = *(LAS v4u*)(L + L_HX + w * 256 + 128 + r8 * 16);
      }
      float v[16];
#pragma unroll
      for (int k = 0; k < 8; ++k) {
        v[k] = dot2(a0[3], wp[24 + k], dot2(a0[2], wp[16 + k], dot2(a0[1], wp[8 + k], dot2(a0[0], wp[k], 0.f))));
        v[8 + k] = dot2(a1[3], wp[24 + k], dot2(a1[2], wp[16 + k], dot2(a1[1], wp[8 + k], dot2(a1[0], wp[k], 0.f))));
      }
#pragma unroll
      for (int k = 0; k < 8; ++k) v[k] = tr32(v[k], v[k + 8]);
      f32x4 y;
#pragma unroll
      for (int k = 0; k < 4; ++k) { const float t = tr16(v[k], v[k + 4]); y[k] = t + dpp<0x128>(t); }
      if (n < 8) *(LAS f32x4*)(L + L_RED + w * 512 + (g >> 1) * 256 + n * 32 + (g & 1) * 16) = y;
      asm volatile("s_waitcnt lgkmcnt(0)" ::: "memory");
      unsigned old = 0;
      if (lane == 0) old = __hip_atomic_fetch_add((LAS unsigned*)(L + L_JCNT), 1u, __ATOMIC_RELAXED, __HIP_MEMORY_SCOPE_WORKGROUP);
      if (__builtin_amdgcn_readfirstlane(old) == (unsigned)(nact - 1)) {
        asm volatile("" : "+v"(ep) :: "memory");
        const unsigned tag = ep + 1u;
        f32x2 s = {0.f, 0.f};
#pragma unroll
        for (int ww = 0; ww < 8 / FH; ++ww) s += *(LAS f32x2*)(L + L_RED + ww * 512 + lane * 8);
        if (isH) {
          GAS u64* o = (GAS u64*)(p.ws + W_HG + (hl & 7) * (2048 * FH) + ((hl >> 3) * FH + hf) * 1024 + (lane & 31) * 32 + (lane >> 5) * 8);
          __hip_atomic_store(o, ((u64)tag << 32) | __float_as_uint(s[0]), RLX_AGENT);
          __hip_atomic_store(o + 2, ((u64)tag << 32) | __float_as_uint(s[1]), RLX_AGENT);
        } else {
#pragma unroll
          for (int ww = 8 / FH; ww < 8; ++ww) s += *(LAS f32x2*)(L + L_RED + ww * 512 + lane * 8);
          const int o = (isQ ? W_QG + (lane >> 5) * 16384 + (bid - Q0) * 256 : W_MG + (lane >> 5) * 8192 + (bid - M0) * 256) + (lane & 31) * 8;
          __hip_atomic_store((GAS u64*)(p.ws + o), ((u64)tag << 32) | cvtpk(s[0], s[1]), RLX_AGENT);
        }
        *(volatile LAS unsigned*)(L + L_JCNT) = 9u;
        STAMP(3);
      }
    }
    asm volatile("" : "+v"(ep));
    const unsigned tag = ep + 1u;
    if (lane == 0) __hip_atomic_fetch_add((LAS unsigned*)(L + L_ECNT), 1u, __ATOMIC_RELAXED, __HIP_MEMORY_SCOPE_WORKGROUP);
    if (w == 7) {
      if (isQ) lds_wait<2>(L, L_JCNT, 9); else lds_wait<4>(L, L_FULL, 8 * QTHR);
      v4u t0, t1;
#pragma unroll 1
      for (unsigned spins = 0;;) {
        if (QSTAG) __builtin_amdgcn_s_sleep(QSTAG);
        PSTAMP(10);
        t0 = LD128(rw, lane * 16, W_QG + b * 16384 + kvh * 2048, AUX_SC1); t1 = LD128(rw, lane * 16 + 1024, W_QG + b * 16384 + kvh * 2048, AUX_SC1);
        if (__all((t0[1] == tag) & (t0[3] == tag) & (t1[1] == tag) & (t1[3] == tag)) || ++spins > SPIN_G) break;
        asm volatile("" ::: "memory");
      }
      *(LAS v2u*)(L + L_Q + lane * 8) = v2u{t0[0], t0[2]}; *(LAS v2u*)(L + L_Q + 512 + lane * 8) = v2u{t1[0], t1[2]};
      asm volatile("s_waitcnt lgkmcnt(0)" ::: "memory");
      *(volatile LAS unsigned*)(L + L_QRDY) = 1u;
      STAMP(4);
    }
    v4u wo_r[4], w2_r[2]; v2u w1_r[4];
    {
      const rsrc_t ro = mkrs(p.wo, (ABL & 1) ? 0 : 4096 * 512 * 2), r1 = mkrs(p.w1, hasC && !(ABL & 2) ? 512 * 1792 * 2 : 0), r2 = mkrs(p.w2, hasC && !(ABL & 4) ? 1792 * 512 * 2 : 0);
#define WT(i) (8 * WBASE + (WORD ? (10 * w + (i)) : (8 * (i) + w)) * WSP)
#pragma unroll
      for (int i = 0; i < 4; ++i) { lds_wait<8>(L, L_FULL, WT(i)); wo_r[i] = LD128(ro, (4 * n + i) * 1024 + g * 16, (kvh * 512 + w * 64) * 1024 + cblk * 64, AUX_W); }
#pragma unroll
      for (int i = 0; i < 4; ++i) { lds_wait<8>(L, L_FULL, WT(4 + i)); w1_r[i] = LD64(r1, (4 * n + i) * 3584 + g * 8, w * 64 * 3584 + fblk * 32, AUX_W); }
#pragma unroll
      for (int i = 0; i < 2; ++i) { lds_wait<8>(L, L_FULL, WT(8 + i)); w2_r[i] = LD128(r2, (2 * r8 + i) * 1024 + c8 * 16, fblk * 16384 + w * 128, AUX_W); }
    }
    const v2u xin = LD64(mkrs(p.x, 2048), n * 8, b * 1024 + w * 128, 0);
    v4u mg[4];
#pragma unroll 1
    for (unsigned spins = 0;;) {
      bool ok = true;
#pragma unroll
      for (int k = 0; k < 3; ++k) mg[k] = LD128(rw, n * 16 + k * 2048, W_MG + b * 8192 + w * 256, AUX_SC1);
      mg[3] = LD128(rw, 0, W_MG + b * 8192 + 6144 + ocol * 4, AUX_SC1);
      const v2u e = LD64(rw, (lane & 31) * 8, W_CL + w * 256, AUX_SC1);
#pragma unroll
      for (int k = 0; k < 4; ++k) ok &= (mg[k][1] == tag) & (mg[k][3] == tag);
      ok &= (e[0] == (tag ^ 0x5bd1e995u)) & (e[1] == tag);
      if (__all(ok) || ++spins > SPIN_G) break;
      __builtin_amdgcn_s_sleep(32); asm volatile("" ::: "memory");
    }
    unsigned wo_p[16], w1_p[8], w2_p[8];
#pragma unroll
    for (int pp = 0; pp < 2; ++pp) {
#pragma unroll
      for (int d = 0; d < 4; ++d) { wo_p[pp * 8 + 2 * d] = pk_lo(wo_r[2 * pp][d], wo_r[2 * pp + 1][d]); wo_p[pp * 8 + 2 * d + 1] = pk_hi(wo_r[2 * pp][d], wo_r[2 * pp + 1][d]); }
#pragma unroll
      for (int d = 0; d < 2; ++d) { w1_p[pp * 4 + 2 * d] = pk_lo(w1_r[2 * pp][d], w1_r[2 * pp + 1][d]); w1_p[pp * 4 + 2 * d + 1] = pk_hi(w1_r[2 * pp][d], w1_r[2 * pp + 1][d]); }
    }
#pragma unroll
    for (int d = 0; d < 4; ++d) { w2_p[2 * d] = pk_lo(w2_r[0][d], w2_r[1][d]); w2_p[2 * d + 1] = pk_hi(w2_r[0][d], w2_r[1][d]); }
    f32x4 xf = {lo2f(xin[0]), hi2f(xin[0]), lo2f(xin[1]), hi2f(xin[1])}, ga = {lo2f(mg[0][0]), hi2f(mg[0][0]), lo2f(mg[0][2]), hi2f(mg[0][2])};
    f32x4 sb = {lo2f(mg[1][0]), hi2f(mg[1][0]), lo2f(mg[1][2]), hi2f(mg[1][2])};
    f32x4 cb1 = {rbf(1.f + lo2f(mg[2][0])), rbf(1.f + hi2f(mg[2][0])), rbf(1.f + lo2f(mg[2][2])), rbf(1.f + hi2f(mg[2][2]))};
    const unsigned gw = (g & 2) ? mg[3][2] : mg[3][0];
    float gv = (g & 1) ? hi2f(gw) : lo2f(gw);
#pragma unroll
    for (int k = 0; k < 16; k += 8) asm volatile("" : "+v"(wo_p[k]), "+v"(wo_p[k + 1]), "+v"(wo_p[k + 2]), "+v"(wo_p[k + 3]), "+v"(wo_p[k + 4]), "+v"(wo_p[k + 5]), "+v"(wo_p[k + 6]), "+v"(wo_p[k + 7]));
    asm volatile("" : "+v"(w1_p[0]), "+v"(w1_p[1]), "+v"(w1_p[2]), "+v"(w1_p[3]), "+v"(w1_p[4]), "+v"(w1_p[5]), "+v"(w1_p[6]), "+v"(w1_p[7]));
    asm volatile("" : "+v"(w2_p[0]), "+v"(w2_p[1]), "+v"(w2_p[2]), "+v"(w2_p[3]), "+v"(w2_p[4]), "+v"(w2_p[5]), "+v"(w2_p[6]), "+v"(w2_p[7]));
    asm volatile("" : "+v"(xf), "+v"(ga), "+v"(sb), "+v"(cb1), "+v"(gv));

    int passv = (DRYRUN && w == 0) ? 0 : 1;
    for (;;) {
      asm volatile("" : "+v"(passv));
      const int pass = __builtin_amdgcn_readfirstlane(passv);
      const rsrc_t rt = mkrs(p.ws, pass ? W_END : 0);
      if (pass) { STAMP(5); bar(); STAMP(12); }
      {
        v4u o4[4]; v2u ml[4];
        const int po = W_PO + (bid & ~15) * 2048 + w * 256, mo = W_ML + (bid & ~15) * 128 + (w >> 1) * 16;
#pragma unroll
        for (int i = 0; i < 4; ++i) { o4[i] = LD128(rt, (4 * i + g) * 2048 + n * 16, po, AUX_SC1); ml[i] = LD64(rt, (4 * i + g) * 128, mo, AUX_SC1); }
#pragma unroll 1
        for (unsigned spins = 0;;) {
          bool bad[4];
#pragma unroll
          for (int i = 0; i < 4; ++i) bad[i] = max(umax4(o4[i]), max(ml[i][0], ml[i][1])) == SENT;
          if (!__any(bad[0] | bad[1] | bad[2] | bad[3]) || ++spins > SPIN_G) break;
          if (POLL_SLEEP) __builtin_amdgcn_s_sleep(POLL_SLEEP);
          asm volatile("" ::: "memory");
#pragma unroll
          for (int i = 0; i < 4; ++i) if (bad[i]) { o4[i] = LD128(rt, (4 * i + g) * 2048 + n * 16, po, AUX_SC1); ml[i] = LD64(rt, (4 * i + g) * 128, mo, AUX_SC1); }
        }
        if (pass) STAMP(6);
        float mi[4], M;
#pragma unroll
        for (int i = 0; i < 4; ++i) mi[i] = __uint_as_float(ml[i][0]);
        M = xg_max(fmaxf(fmaxf(mi[0], mi[1]), fmaxf(mi[2], mi[3])));
        float Ls = 0.f; f32x4 o = {0.f, 0.f, 0.f, 0.f};
#pragma unroll
        for (int i = 0; i < 4; ++i) { const float e = __builtin_amdgcn_exp2f((mi[i] - M) * L2E); Ls += e * __uint_as_float(ml[i][1]); o += e * __builtin_bit_cast(f32x4, o4[i]); }
        const float inv = rcp(xg_sum(Ls));
        const unsigned p0 = cvtpk(xg_sum(o[0]) * inv, xg_sum(o[1]) * inv), p1 = cvtpk(xg_sum(o[2]) * inv, xg_sum(o[3]) * inv);
        float y[8];
#pragma unroll
        for (int k = 0; k < 8; ++k) y[k] = row_sum(dot2(p1, wo_p[8 + k], dot2(p0, wo_p[k], 0.f)));
        if (n < 2) *(LAS f32x4*)(L + L_RAB + w * 128 + g * 32 + n * 16) = n ? f32x4{y[4], y[5], y[6], y[7]} : f32x4{y[0], y[1], y[2], y[3]};
        if (pass) { STAMP(13); bar(); }
        if (w == 0) {
          f32x4 s = *(LAS f32x4*)(L + L_RAB + r8 * 128 + c8 * 16);
#pragma unroll
          for (int k = 0; k < 4; ++k) { float t = s[k]; t += dpp<0x128>(t); s[k] = xg_sum(t); }
          const v4u su = __builtin_bit_cast(v4u, s);
          ST128((v4u{nosent(su[0]), nosent(su[1]), nosent(su[2]), nosent(su[3])}), rt, lane < 8 ? lane * 16 : (int)OOB, W_AB + ((b * 8 + kvh) * 512 + cblk * 32) * 4, AUX_SC1);
          if (pass) STAMP(7);
        }
      }
      {
        v4u a[2];
#pragma unroll
        for (int i = 0; i < 2; ++i) a[i] = LD128(rt, (4 * i + g) * 2048 + n * 16, W_AB + b * 16384 + w * 256, AUX_SC1);
#pragma unroll 1
        for (unsigned spins = 0;;) {
          const bool bad0 = umax4(a[0]) == SENT, bad1 = umax4(a[1]) == SENT;
          if (!__any(bad0 | bad1) || ++spins > SPIN_G) break;
          if (POLL_SLEEP) __builtin_amdgcn_s_sleep(POLL_SLEEP);
          asm volatile("" ::: "memory");
          if (bad0) a[0] = LD128(rt, g * 2048 + n * 16, W_AB + b * 16384 + w * 256, AUX_SC1);
          if (bad1) a[1] = LD128(rt, (4 + g) * 2048 + n * 16, W_AB + b * 16384 + w * 256, AUX_SC1);
        }
        if (pass) STAMP(8);
        const f32x4 as = __builtin_bit_cast(f32x4, a[0]) + __builtin_bit_cast(f32x4, a[1]);
        f32x4 x1;
#pragma unroll
        for (int k = 0; k < 4; ++k) x1[k] = rbf(xf[k] + rbf(ga[k] * rbf(xg_sum(as[k]))));
        const float pv = __builtin_bit_cast(float, __builtin_amdgcn_readfirstlane(__builtin_bit_cast(int, x1[0])));
        const f32x4 dx = x1 - pv;
        const float s1 = row_sum((dx[0] + dx[1]) + (dx[2] + dx[3])), s2 = row_sum((dx[0] * dx[0] + dx[1] * dx[1]) + (dx[2] * dx[2] + dx[3] * dx[3]));
        if (lane == 0) *(LAS f32x2*)(L + L_ST + w * 8) = f32x2{pv + s1 * (1.f / 64.f), s2 - s1 * s1 * (1.f / 64.f)};
        if (w == xl && lane == j16) *(LAS f32x4*)(L + L_X1) = x1;
        if (pass) { STAMP(14); bar(); }
        f32x4 st[4];
#pragma unroll
        for (int k = 0; k < 4; ++k) st[k] = *(LAS f32x4*)(L + L_ST + k * 16);
        const float mu = (((st[0][0] + st[0][2]) + (st[1][0] + st[1][2])) + ((st[2][0] + st[2][2]) + (st[3][0] + st[3][2]))) * 0.125f;
        float m2 = 0.f, dv = 0.f;
#pragma unroll
        for (int k = 0; k < 4; ++k) { m2 += st[k][1] + st[k][3]; const float t0 = st[k][0] - mu, t1 = st[k][2] - mu; dv += t0 * t0 + t1 * t1; }
        const float mean = rbf(mu), var = rbf(fmaxf(m2 + 64.f * dv, 0.f) * (1.f / 512.f));
        const float isd = rcp(rbf(__builtin_amdgcn_sqrtf(rbf(var + 1e-5f))));
        float h[4];
#pragma unroll
        for (int k = 0; k < 4; ++k) h[k] = rbf(rbf(rbf(x1[k] - mean) * isd) * cb1[k]) + sb[k];
        const unsigned p0 = cvtpk(h[0], h[1]), p1 = cvtpk(h[2], h[3]);
        f32x4 y;
#pragma unroll
        for (int k = 0; k < 4; ++k) y[k] = row_sum(dot2(p1, w1_p[4 + k], dot2(p0, w1_p[k], 0.f)));
        if (n == 0) *(LAS f32x4*)(L + L_RC + w * 64 + g * 16) = y;
        if (pass) { STAMP(15); bar(); }
        const f32x2 s = *(LAS f32x2*)(L + L_RC + c8 * 64 + r8 * 8);
        float t0 = s[0], t1 = s[1];
        t0 += dpp<0xB1>(t0); t1 += dpp<0xB1>(t1); t0 += dpp<0x4E>(t0); t1 += dpp<0x4E>(t1); t0 += dpp<0x141>(t0); t1 += dpp<0x141>(t1);
        const unsigned d = cvtpk(silu_f(rbf(t0)), silu_f(rbf(t1)));
        float z[8];
#pragma unroll
        for (int k = 0; k < 8; ++k) z[k] = dot2(d, w2_p[k], 0.f);
        v4u o;
#pragma unroll
        for (int k = 0; k < 4; ++k) { float t = tr32(z[k], z[k + 4]); t = tr16(t, t); t += dpp<0x128>(t); o[k] = nosent(__float_as_uint(t)); }
        ST128(o, rt, (lane & 24) == 0 && hasC ? (c8 >> 2) * (112 * 128) + (c8 & 3) * 32 + (lane >> 5) * 16 : (int)OOB, W_OP + ((b * 16 + 2 * w) * 112 + fblk) * 128, AUX_SC1);
        if (pass) TSTAMP(9);
      }
      if (w == 0) {
        const int oo = W_OP + (b * 16 + (ocol >> 5)) * (112 * 128) + (ocol & 31) * 4;
        const int o0 = lane < 56 ? lane * 128 : (int)OOB, o1 = lane < 56 ? (lane + 56) * 128 : (int)OOB;
        const float xv = *(LAS float*)(L + L_X1 + g * 4);
        v4u a0 = LD128(rt, o0, oo, AUX_SC1), a1 = LD128(rt, o1, oo, AUX_SC1);
#pragma unroll 1
        for (unsigned spins = 0;;) {
          const bool bad0 = umax4(a0) == SENT, bad1 = umax4(a1) == SENT;
          if (!__any(bad0 | bad1) || ++spins > SPIN_G) break;
          if (POLL_SLEEP) __builtin_amdgcn_s_sleep(POLL_SLEEP);
          asm volatile("" ::: "memory");
          if (bad0) a0 = LD128(rt, o0, oo, AUX_SC1);
          if (bad1) a1 = LD128(rt, o1, oo, AUX_SC1);
        }
        if (pass) TSTAMP(10);
        const f32x4 y = __builtin_bit_cast(f32x4, a0) + __builtin_bit_cast(f32x4, a1);
        const float y2 = row_sum(tr16(tr32(y[0], y[2]), tr32(y[1], y[3])));
        const unsigned r = cvtpk(xv + rbf(gv * rbf(y2)), 0.f);
        if (pass && n == 0) *(GAS u16*)(p.out + b * 512 + ocol + g) = (u16)r;
        __builtin_amdgcn_raw_buffer_store_b32(tag, rt, (lane == 0 && bid == 0) ? 0 : (int)OOB, W_EPOCH, AUX_SC1);
        if (pass) TSTAMP(11);
      }
      if (pass) break;
      passv = 1;
    }
    STAMP_OUT();
    return;
  }

  {
    const rsrc_t rk = mkrs(p.K + (size_t)bid * (NUNIT * 4096), NUNIT * 8192), rv = mkrs(p.V + (size_t)bid * (NUNIT * 4096), NUNIT * 8192);
    const int k0 = g * 256 + ((n ^ g) * 16), k1 = k0 ^ 64, k2 = k0 ^ 128, k3 = k0 ^ 192, ve = g * 256 + ((n ^ (2 * g)) * 16), vo = ve ^ 128;
    unsigned freed = 0; int slot = 0, pub = 0;
    const int deep = (QHOLD && isQ) ? QCATCH : 0;
    STAMP(3);
#define PACE(done) do { if (u < NSLOW) { waitvm<DSLOW - 4>(); pub = max(pub, (done) - (DSLOW - 4)); } else if (u < deep) { waitvm<DDEEP - 4>(); pub = max(pub, (done) - (DDEEP - 4)); } \
                        else { waitvm<DEPTH - 4>(); pub = max(pub, (done) - (DEPTH - 4)); } *FULL = pub; } while (0)
#pragma unroll 1
    for (int u = 0; u < NUNIT; ++u) {
      if (QHOLD && u == QHOLD && isQ) { waitvm<0>(); pub = 16 * u; *FULL = pub; lds_wait<1>(L, L_JCNT, 9); }
      if (LPRIO && u == 1) __builtin_amdgcn_s_setprio(LPRIO);
      if (u >= NSLOT && freed < (unsigned)(u - NSLOT + 1)) {
        unsigned spins = 0; bool drained = false;
        for (;;) {
          const v4u f = *(volatile LAS v4u*)(L + L_FREE);
          freed = __builtin_amdgcn_readfirstlane(min(min(f[0], f[1]), min(f[2], f[3])));
          if (freed >= (unsigned)(u - NSLOT + 1)) break;
          if (++spins > SPIN_L) { freed = SENT; break; }
          if (!drained) { if (u == NSLOT) STAMP(4); waitvm<0>(); pub = 16 * u; *FULL = pub; drained = true; STAMP(2); }
          __builtin_amdgcn_s_sleep(1);
        }
      }
      const unsigned lb = slot * 16384;
      dma4(rk, k0, k1, k2, k3, u * 8192, lb); PACE(16 * u + 4);
      dma4(rk, k0, k1, k2, k3, u * 8192 + 4096, lb + 4096); PACE(16 * u + 8);
      dma4(rv, ve, vo, ve, vo, u * 8192, lb + 8192); PACE(16 * u + 12);
      dma4(rv, ve, vo, ve, vo, u * 8192 + 4096, lb + 12288); PACE(16 * u + 16);
      slot = (slot + 1 == NSLOT) ? 0 : slot + 1;
    }
    if (DEPTH > 28) { waitvm<24>(); *FULL = 16 * NUNIT - 24; }
    if (DEPTH > 20) { waitvm<16>(); *FULL = 16 * NUNIT - 16; }
    waitvm<8>(); *FULL = 16 * NUNIT - 8;
    waitvm<0>(); *FULL = 16 * NUNIT;
    STAMP(1);
    STAMP_OUT();
  }
}

extern "C" void kernel_launch(void* const* d_in, const int* in_sizes, int n_in, void* d_out, int out_size, void* d_ws, size_t ws_size, hipStream_t stream) {
  Params p;
  p.x = (const u16*)d_in[0]; p.K = (const u16*)d_in[1]; p.V = (const u16*)d_in[2]; p.wq = (const u16*)d_in[3]; p.wo = (const u16*)d_in[4];
  p.w1 = (const u16*)d_in[6]; p.w2 = (const u16*)d_in[7]; p.c = (const u16*)d_in[8]; p.wmod = (const u16*)d_in[9];
  p.out = (u16*)d_out; p.ws = (unsigned char*)d_ws;
  (void)hipFuncSetAttribute((const void*)adaln_block_kernel, hipFuncAttributeMaxDynamicSharedMemorySize, L_END);
  hipLaunchKernelGGL(adaln_block_kernel, dim3(NWG), dim3(NTHR), L_END, stream, p);
}
```

```cpp
#define STAMPS
#define PROBEW 0
#define PROBEK 0
#define PROBEM 0
#include <hip/hip_runtime.h>
#include <hip/hip_bf16.h>
#include <stdint.h>

typedef unsigned short u16;
typedef unsigned long long u64;
typedef unsigned v4u __attribute__((ext_vector_type(4)));
typedef unsigned v2u __attribute__((ext_vector_type(2)));
typedef float f32x4 __attribute__((ext_vector_type(4)));
typedef float f32x2 __attribute__((ext_vector_type(2)));
typedef short bf16x8 __attribute__((ext_vector_type(8)));
typedef short s16x4 __attribute__((ext_vector_type(4)));
typedef __bf16 bf16x2_t __attribute__((ext_vector_type(2)));
typedef __amdgpu_buffer_rsrc_t rsrc_t;
#define GAS __attribute__((address_space(1)))
#define LAS __attribute__((address_space(3)))
#define RLX_AGENT __ATOMIC_RELAXED, __HIP_MEMORY_SCOPE_AGENT

#define NSLOT 9
#define DEPTH 24
#define DRYRUN 1
#define FH 2
#define NSLOW 0
#define DSLOW 16
#define DDEEP 32
#define QHOLD 1
#define QCATCH 24
#define CODEWARM 1
#define AUX_W 0
#define ABL 0
#define WSP 8
#define WORD 1
#define WBASE 20
#define LPRIO 2
#define POLL_SLEEP 0
#define HSTAG 40
#define QTHR 9
#define QSTAG 4

constexpr int NWG = 256, NWAVE = 13, NTHR = NWAVE * 64, NUNIT = 64;
constexpr int NH = 16 * FH, Q0 = NH, M0 = Q0 + 64, JEND = M0 + 32;
constexpr unsigned SENT = 0xFFFFFFFFu, OOB = 0x7FFFFFF0u;
constexpr int AUX_NT = 2, AUX_SC1 = 16;
constexpr int W_EPOCH = 0, W_CL = 0x800, W_QG = 0x2000  , W_MG = 0xA000  , W_AB = 0x10000, W_ML = 0x18000,
              W_PO = 0x20000, W_OP = 0xA0000  , W_HG = 0x110000  , W_END = 0x120000;
constexpr int W_STAMP = 0x200000;
constexpr int L_CTRL = NSLOT * 16384, L_FULL = L_CTRL, L_QRDY = L_CTRL + 4, L_JCNT = L_CTRL + 8, L_ECNT = L_CTRL + 12, L_FREE = L_CTRL + 16, L_Q = L_CTRL + 64,
              L_RED = L_Q + 1024  , L_HX = L_RED + 4096  , L_RAB = L_HX + 2048, L_ST = L_RAB + 1024, L_RC = L_ST + 64, L_X1 = L_RC + 512,
              L_STAMP = L_X1 + 16, L_END = L_STAMP + NWAVE * 128;
static_assert(L_END <= 160 * 1024, "LDS");
constexpr unsigned SPIN_G = 1u << 17, SPIN_L = 1u << 22;

#define STAMP(k) do { const u64 t_ = __builtin_amdgcn_s_memrealtime(); if (lane == 0) *(volatile LAS u64*)(L + L_STAMP + w * 128 + (k) * 8) = t_; } while (0)
#define TSTAMP(k) STAMP(k)
#define PSTAMP(k) do {} while (0)
#define STAMP_INIT() do { const u64 t_ = __builtin_amdgcn_s_memrealtime(); if (lane < 16) *(volatile LAS u64*)(L + L_STAMP + w * 128 + lane * 8) = lane ? 0ull : t_; } while (0)
#define STAMP_OUT() do {} while (0)

__device__ __forceinline__ float lo2f(unsigned u) { return __builtin_bit_cast(float, u << 16); }
__device__ __forceinline__ float hi2f(unsigned u) { return __builtin_bit_cast(float, u & 0xffff0000u); }
__device__ __forceinline__ unsigned cvtpk(float lo, float hi) { f32x2 v = {lo, hi}; return __builtin_bit_cast(unsigned, __builtin_convertvector(v, bf16x2_t)); }
__device__ __forceinline__ float rbf(float x) { return lo2f(cvtpk(x, x)); }
__device__ __forceinline__ float rcp(float x) { return __builtin_amdgcn_rcpf(x); }
__device__ __forceinline__ float silu_f(float x) { return x * rcp(1.f + __builtin_amdgcn_exp2f(x * -1.4426950408889634f)); }
__device__ __forceinline__ f32x4 mfma(v4u a, v4u b, f32x4 c) {
  return __builtin_amdgcn_mfma_f32_16x16x32_bf16(__builtin_bit_cast(bf16x8, a), __builtin_bit_cast(bf16x8, b), c, 0, 0, 0);
}
__device__ __forceinline__ float dot2(unsigned a, unsigned b, float c) {
  return __builtin_amdgcn_fdot2_f32_bf16(__builtin_bit_cast(bf16x2_t, a), __builtin_bit_cast(bf16x2_t, b), c, false);
}
__device__ __forceinline__ unsigned pk_lo(unsigned r0, unsigned r1) { return __builtin_amdgcn_perm(r1, r0, 0x05040100u); }
__device__ __forceinline__ unsigned pk_hi(unsigned r0, unsigned r1) { return __builtin_amdgcn_perm(r1, r0, 0x07060302u); }
__device__ __forceinline__ unsigned nosent(unsigned d) { return d < 0xFFFFFFFEu ? d : 0xFFFFFFFEu; }
template <int CTRL> __device__ __forceinline__ float dpp(float x) {
  return __builtin_bit_cast(float, __builtin_amdgcn_mov_dpp(__builtin_bit_cast(int, x), CTRL, 0xf, 0xf, true));
}
__device__ __forceinline__ float row_sum(float x) { x += dpp<0x128>(x); x += dpp<0x124>(x); x += dpp<0x122>(x); x += dpp<0x121>(x); return x; }
__device__ __forceinline__ float tr32(float a, float b) {
  auto s = __builtin_amdgcn_permlane32_swap(__float_as_uint(a), __float_as_uint(b), false, false);
  return __uint_as_float(s[0]) + __uint_as_float(s[1]);
}
__device__ __forceinline__ float tr16(float a, float b) {
  auto s = __builtin_amdgcn_permlane16_swap(__float_as_uint(a), __float_as_uint(b), false, false);
  return __uint_as_float(s[0]) + __uint_as_float(s[1]);
}
__device__ __forceinline__ float xg_sum(float x) { x = tr16(x, x); return tr32(x, x); }
__device__ __forceinline__ float xg_max(float x) {
  auto s = __builtin_amdgcn_permlane16_swap(__float_as_uint(x), __float_as_uint(x), false, false);
  x = fmaxf(__uint_as_float(s[0]), __uint_as_float(s[1]));
  auto t = __builtin_amdgcn_permlane32_swap(__float_as_uint(x), __float_as_uint(x), false, false);
  return fmaxf(__uint_as_float(t[0]), __uint_as_float(t[1]));
}
__device__ __forceinline__ float wave_sum(float x) { return xg_sum(row_sum(x)); }
__device__ __forceinline__ unsigned umax4(v4u v) { return max(max(v[0], v[1]), max(v[2], v[3])); }
template <int N> __device__ __forceinline__ void waitvm() { asm volatile("s_waitcnt vmcnt(%0)" :: "n"(N) : "memory"); }
__device__ __forceinline__ void bar() { asm volatile("s_waitcnt lgkmcnt(0)\n\ts_barrier" ::: "memory"); }
__device__ __forceinline__ rsrc_t mkrs(const void* p, unsigned bytes) { return __builtin_amdgcn_make_buffer_rsrc((void*)p, 0, bytes, 0x00020000); }
__device__ __forceinline__ void dma4(rsrc_t rs, int v0, int v1, int v2, int v3, int soff, unsigned lds) {
  asm volatile("s_mov_b32 m0, %6\n\ts_nop 0\n\tbuffer_load_dwordx4 %0, %4, %5 offen nt lds\n\tbuffer_load_dwordx4 %1, %4, %5 offen offset:1024 nt lds\n\t"
               "buffer_load_dwordx4 %2, %4, %5 offen offset:2048 nt lds\n\tbuffer_load_dwordx4 %3, %4, %5 offen offset:3072 nt lds"
               :: "v"(v0), "v"(v1), "v"(v2), "v"(v3), "s"(rs), "s"(soff), "s"(lds) : "memory");
}
#define LD128(rs, vo, so, aux) __builtin_amdgcn_raw_buffer_load_b128(rs, vo, so, aux)
#define LD64(rs, vo, so, aux) __builtin_amdgcn_raw_buffer_load_b64(rs, vo, so, aux)
#define LD32(rs, vo, so, aux) __builtin_amdgcn_raw_buffer_load_b32(rs, vo, so, aux)
#define ST128(v, rs, vo, so, aux) __builtin_amdgcn_raw_buffer_store_b128(v, rs, vo, so, aux)
#define ST64(v, rs, vo, so, aux) __builtin_amdgcn_raw_buffer_store_b64(v, rs, vo, so, aux)

struct Params { const u16 *x, *K, *V, *wq, *wo, *w1, *w2, *c, *wmod; u16* out; unsigned char* ws; };

__device__ __forceinline__ unsigned lds_get(LAS unsigned char* L, int off) { return __builtin_amdgcn_readfirstlane(*(volatile LAS unsigned*)(L + off)); }
template <int SL> __device__ __forceinline__ void lds_wait(LAS unsigned char* L, int off, unsigned want) {
#pragma unroll 1
  for (unsigned spins = 0; lds_get(L, off) < want && ++spins < SPIN_L;) __builtin_amdgcn_s_sleep(SL);
  asm volatile("" ::: "memory");
}

__global__ void __launch_bounds__(NTHR, 4) __attribute__((aligned(4096))) adaln_block_kernel(Params p) {
  extern __shared__ __attribute__((aligned(16))) unsigned char smem[];
  LAS unsigned char* L = (LAS unsigned char*)smem;
  const int tid = threadIdx.x, lane = tid & 63, w = __builtin_amdgcn_readfirstlane(tid >> 6);
  const int bid = blockIdx.x;
  unsigned cw[5] = {0u, 0u, 0u, 0u, 0u};
  if (CODEWARM && w == 9 && (CODEWARM == 2 || (bid >> 4) == 8)) {
    u64 pc0; unsigned clen;
    asm volatile("s_getpc_b64 %0\n.Ladaln_code_begin:\n\ts_mov_b32 %1, .Ladaln_code_end-.Ladaln_code_begin" : "=s"(pc0), "=s"(clen));
    const rsrc_t rc = mkrs((const void*)pc0, clen);
#pragma unroll
    for (int i = 0; i < 5; ++i) cw[i] = LD32(rc, lane * 64, i * 4096, 0);
  }
  STAMP_INIT();
  if (tid < 16) ((LAS unsigned*)(L + L_CTRL))[tid] = 0u;
  asm volatile("s_waitcnt lgkmcnt(0)\n\ts_barrier" : "+s"(p.x), "+s"(p.K), "+s"(p.V), "+s"(p.wq), "+s"(p.wo), "+s"(p.w1), "+s"(p.w2), "+s"(p.c), "+s"(p.wmod), "+s"(p.out), "+s"(p.ws) :: "memory");
  const int xl = bid & 7, jl = bid >> 3, b = bid >> 7, kvh = (bid >> 4) & 7, mem = bid & 15;
  const int g = lane >> 4, n = lane & 15, r8 = lane >> 3, c8 = lane & 7;
  volatile LAS unsigned* FULL = (volatile LAS unsigned*)(L + L_FULL);
  constexpr float L2E = 1.4426950408889634f;
  const bool isH = bid < Q0, isQ = !isH && bid < M0;
  const rsrc_t rw = mkrs(p.ws, W_END);
  const int cblk = 2 * ((mem + kvh) & 7) + (mem >> 3);
  const int j16 = jl & 15;
  const bool hasC = j16 < 14;
  const int fblk = xl * 14 + j16;
  const int ocol = xl * 64 + j16 * 4;

  if (w >= 8 && w < 12) {
  const int c = w - 8;
  asm volatile("" :: "v"(cw[0]), "v"(cw[1]), "v"(cw[2]), "v"(cw[3]), "v"(cw[4]));
  if (c == 0) {
    unsigned ep = LD32(rw, 0, W_EPOCH, AUX_SC1);
    lds_wait<4>(L, L_FULL, 16);
    asm volatile("" : "+v"(ep));
    const unsigned tag = ep + 1u;
    const v4u s4 = {SENT, SENT, SENT, SENT};
    ST128(s4, rw, lane * 16, W_PO + bid * 2048, AUX_SC1);
    ST128(s4, rw, lane * 16 + 1024, W_PO + bid * 2048, AUX_SC1);
    const int oo = hasC ? W_OP + (b * 16 * 112 + fblk) * 128 : (int)OOB;
    ST128(s4, rw, r8 * (112 * 128) + c8 * 16, oo, AUX_SC1);
    ST128(s4, rw, (8 + r8) * (112 * 128) + c8 * 16, oo, AUX_SC1);
    ST128(s4, rw, lane < 4 ? W_ML + bid * 128 + lane * 16 : lane < 12 ? W_AB + ((b * 8 + kvh) * 512 + cblk * 32) * 4 + (lane - 4) * 16 : (int)OOB, 0, AUX_SC1);
    waitvm<0>();
    lds_wait<2>(L, L_ECNT, 8);
    if (lane == 0) __hip_atomic_store((GAS u64*)(p.ws + W_CL + bid * 8), ((u64)tag << 32) | (tag ^ 0x5bd1e995u), RLX_AGENT);
    STAMP(2);
  }
  f32x4 acc0 = {0.f, 0.f, 0.f, 0.f}, acc1 = {0.f, 0.f, 0.f, 0.f};
  float m = -INFINITY, l = 0.f;
  lds_wait<1>(L, L_QRDY, 1);
  STAMP(1);
  v4u qf[4];
#pragma unroll
  for (int s = 0; s < 4; ++s) qf[s] = *(LAS v4u*)(L + L_Q + (n & 3) * 256 + s * 64 + g * 16);
  const int r7 = 4 * (g & 1) + (n >> 2);
  const int va0 = 8192 + (4 * g + (n >> 2)) * 256 + (((4 * c + ((n >> 1) & 1)) ^ (2 * r7)) * 16) + 8 * (n & 1), va1 = va0 ^ 32;
  int po = W_PO + bid * 2048 + (n & 3) * 512 + c * 128 + (n >> 2) * 64 + g * 16;
  if (n >= 8) po = (n < 12 && g == 0 && c == 0) ? W_ML + bid * 128 + (n & 3) * 16 : (int)OOB;
  asm volatile("" : "+v"(po));
  const int ka = n * 256 + ((g ^ n) * 16);
  unsigned full = 0; int slot = 0;
  float lt = 0.f, mb = 0.f;
  constexpr float SC = 0.08838834764831845f;
#pragma unroll 1
  for (int u = 0; u < NUNIT; ++u) {
    LAS unsigned char* S = L + slot * 16384;
    for (unsigned spins = 0; full < (unsigned)(16 * u + 8);) { full = __builtin_amdgcn_readfirstlane(*FULL); if (++spins > SPIN_L) full = SENT; }
    asm volatile("" ::: "memory");
    v4u kf[8];
#pragma unroll
    for (int i = 0; i < 8; ++i) kf[i] = *(LAS v4u*)(S + (ka ^ ((i & 3) * 64)) + (i >> 2) * 4096);
    __builtin_amdgcn_sched_barrier(0);
    f32x4 s0 = {0.f, 0.f, 0.f, 0.f}, s1 = {0.f, 0.f, 0.f, 0.f};
#pragma unroll
    for (int s = 0; s < 4; ++s) { s0 = mfma(kf[s], qf[s], s0); s1 = mfma(kf[4 + s], qf[s], s1); }
    const unsigned u0 = cvtpk(s0[0], s0[1]), u1 = cvtpk(s0[2], s0[3]), u2 = cvtpk(s1[0], s1[1]), u3 = cvtpk(s1[2], s1[3]);
    const unsigned us = (n & 8) ? ((n & 4) ? u3 : u2) : ((n & 4) ? u1 : u0);
    const unsigned ts = cvtpk(lo2f(us) * SC, hi2f(us) * SC);
    const float e0 = lo2f(ts), e1 = hi2f(ts);
    float bm = fmaxf(e0, e1);
    if (__any(bm > m)) {
      bm = fmaxf(bm, dpp<0x124>(bm)); bm = fmaxf(bm, dpp<0x128>(bm));
      const float mn = fmaxf(m, xg_max(bm));
      const float alpha = __builtin_amdgcn_exp2f((m - mn) * L2E);
      m = mn; mb = mn * L2E; acc0 *= alpha; acc1 *= alpha; l *= alpha;
    }
    const float p0 = __builtin_amdgcn_exp2f(e0 * L2E - mb), p1 = __builtin_amdgcn_exp2f(e1 * L2E - mb);
    l += p0 + p1;
    const int pp = (int)cvtpk(p0, p1);
    v4u pf = {(unsigned)pp, (unsigned)__builtin_amdgcn_mov_dpp(pp, 0x12C, 0xf, 0xf, true), (unsigned)__builtin_amdgcn_mov_dpp(pp, 0x128, 0xf, 0xf, true), (unsigned)__builtin_amdgcn_mov_dpp(pp, 0x124, 0xf, 0xf, true)};
    if (u == NUNIT - 1) { lt = l + dpp<0x124>(l); lt += dpp<0x128>(lt); lt = xg_sum(lt); STAMP(3); }
    asm volatile("" : "+v"(pf), "+v"(acc0), "+v"(acc1), "+v"(l), "+v"(lt));
    for (unsigned spins = 0; full < (unsigned)(16 * u + 16);) { full = __builtin_amdgcn_readfirstlane(*FULL); if (++spins > SPIN_L) full = SENT; }
    asm volatile("" ::: "memory");
    const v2u a00 = __builtin_bit_cast(v2u, __builtin_amdgcn_ds_read_tr16_b64_v4i16((LAS s16x4*)(S + va0)));
    const v2u a01 = __builtin_bit_cast(v2u, __builtin_amdgcn_ds_read_tr16_b64_v4i16((LAS s16x4*)(S + va0 + 4096)));
    const v2u a10 = __builtin_bit_cast(v2u, __builtin_amdgcn_ds_read_tr16_b64_v4i16((LAS s16x4*)(S + va1)));
    const v2u a11 = __builtin_bit_cast(v2u, __builtin_amdgcn_ds_read_tr16_b64_v4i16((LAS s16x4*)(S + va1 + 4096)));
    asm volatile("s_waitcnt lgkmcnt(0)" ::: "memory");
    *(volatile LAS unsigned*)(L + L_FREE + c * 4) = (unsigned)(u + 1);
    acc0 = mfma(v4u{a00[0], a00[1], a01[0], a01[1]}, pf, acc0);
    acc1 = mfma(v4u{a10[0], a10[1], a11[0], a11[1]}, pf, acc1);
    slot = (slot + 1 == NSLOT) ? 0 : slot + 1;
  }
  {
    f32x4 v = acc0;
#pragma unroll
    for (int k = 0; k < 4; ++k) { const float t = dpp<0x124>(acc1[k]); v[k] = n < 4 ? v[k] : t; }
    if (n >= 8) v = f32x4{m, lt, m, lt};
    const v4u vu = __builtin_bit_cast(v4u, v);
    ST128((v4u{nosent(vu[0]), nosent(vu[1]), nosent(vu[2]), nosent(vu[3])}), rw, po, 0, AUX_SC1);
  }
  STAMP(5);
  bar();
  asm volatile(".Ladaln_code_end:");
  STAMP_OUT();
  return;
  }

  if (w < 8) {
    unsigned ep = LD32(rw, 0, W_EPOCH, AUX_SC1);
    const int nact = isH ? 8 / FH : 8;
    if (bid < JEND && w < nact) {
      const int hl = bid & 15, hf = bid >> 4;
      const int pitch = isQ ? 8192 : 6144;
      const int cb = (isH ? hl : isQ ? bid - Q0 : bid - M0 + 16) * 128;
      const int row0 = (isH ? hf * (512 / FH) : 0) + w * 64;
      const rsrc_t rj = mkrs(isQ ? p.wq : p.wmod, 512 * pitch), ri = mkrs(isQ ? p.x : p.c, 2048);
      const int io = isQ ? lane * 16 : row0 * 2 + r8 * 16;
      const v4u i0 = LD128(ri, io, 0, 0), i1 = LD128(ri, io, 1024, 0);
      unsigned xd0 = __builtin_amdgcn_raw_buffer_load_b16(ri, lane * 2, w * 128, 0), xd1 = __builtin_amdgcn_raw_buffer_load_b16(ri, lane * 2, 1024 + w * 128, 0);
      v4u wr[8];
#pragma unroll
      for (int i = 0; i < 8; ++i) wr[i] = LD128(rj, r8 * 8 * pitch + c8 * 16, (row0 + i) * pitch + cb, AUX_NT);
      __builtin_amdgcn_sched_barrier(0);
      STAMP(9);
      v4u a0, a1;
      float ln0 = 0.f, ln1 = 0.f;
      if (!isQ) {
#pragma unroll
        for (int k = 0; k < 4; ++k) { a0[k] = cvtpk(silu_f(lo2f(i0[k])), silu_f(hi2f(i0[k]))); a1[k] = cvtpk(silu_f(lo2f(i1[k])), silu_f(hi2f(i1[k]))); }
      } else {
        a0 = i0; a1 = i1;
        asm volatile("" : "+v"(xd0), "+v"(xd1));
        float sm0 = 0.f, sm1 = 0.f;
#pragma unroll
        for (int k = 0; k < 4; ++k) { sm0 += lo2f(i0[k]) + hi2f(i0[k]); sm1 += lo2f(i1[k]) + hi2f(i1[k]); }
        const float mean0 = wave_sum(sm0) * (1.f / 512.f), mean1 = wave_sum(sm1) * (1.f / 512.f);
        float q0 = 0.f, q1 = 0.f;
#pragma unroll
        for (int k = 0; k < 4; ++k) {
          float t = lo2f(i0[k]) - mean0; q0 += t * t; t = hi2f(i0[k]) - mean0; q0 += t * t;
          t = lo2f(i1[k]) - mean1; q1 += t * t; t = hi2f(i1[k]) - mean1; q1 += t * t;
        }
        const float isd0 = rcp(rbf(__builtin_amdgcn_sqrtf(rbf(rbf(wave_sum(q0) * (1.f / 512.f)) + 1e-5f))));
        const float isd1 = rcp(rbf(__builtin_amdgcn_sqrtf(rbf(rbf(wave_sum(q1) * (1.f / 512.f)) + 1e-5f))));
        ln0 = rbf(rbf(lo2f(xd0) - rbf(mean0)) * isd0); ln1 = rbf(rbf(lo2f(xd1) - rbf(mean1)) * isd1);
      }
      asm volatile("" : "+v"(a0), "+v"(a1), "+v"(ln0), "+v"(ln1));
      unsigned wp[32];
#pragma unroll
      for (int pp = 0; pp < 4; ++pp)
#pragma unroll
        for (int d = 0; d < 4; ++d) { wp[pp * 8 + 2 * d] = pk_lo(wr[2 * pp][d], wr[2 * pp + 1][d]); wp[pp * 8 + 2 * d + 1] = pk_hi(wr[2 * pp][d], wr[2 * pp + 1][d]); }
#pragma unroll
      for (int k = 0; k < 32; k += 8) asm volatile("" : "+v"(wp[k]), "+v"(wp[k + 1]), "+v"(wp[k + 2]), "+v"(wp[k + 3]), "+v"(wp[k + 4]), "+v"(wp[k + 5]), "+v"(wp[k + 6]), "+v"(wp[k + 7]));
      STAMP(1);
      if (isQ) {
        asm volatile("" : "+v"(ep));
        const unsigned tag = ep + 1u;
        v4u t[2 * FH];
        if (HSTAG) __builtin_amdgcn_s_sleep(HSTAG);
#pragma unroll 1
        for (unsigned spins = 0;;) {
          bool ok = true;
          PSTAMP(11);
#pragma unroll
          for (int k = 0; k < 2 * FH; ++k) t[k] = LD128(rw, lane * 16 + k * 1024, W_HG + w * (2048 * FH), AUX_SC1);
#pragma unroll
          for (int k = 0; k < 2 * FH; ++k) ok &= (t[k][1] == tag) & (t[k][3] == tag);
          if (__all(ok) || ++spins > SPIN_G) break;
          asm volatile("" ::: "memory");
        }
        STAMP(2);
        float sa0 = 0.f, sa1 = 0.f, ca0 = 0.f, ca1 = 0.f;
#pragma unroll
        for (int k = 0; k < FH; ++k) { sa0 += __uint_as_float(t[k][0]); sa1 += __uint_as_float(t[k][2]); ca0 += __uint_as_float(t[FH + k][0]); ca1 += __uint_as_float(t[FH + k][2]); }
        const unsigned hh = cvtpk(rbf(ln0 * rbf(1.f + rbf(ca0))) + rbf(sa0), rbf(ln1 * rbf(1.f + rbf(ca1))) + rbf(sa1));
        *(LAS u16*)(L + L_HX + w * 256 + lane * 2) = (u16)hh; *(LAS u16*)(L + L_HX + w * 256 + 128 + lane * 2) = (u16)(hh >> 16);
        asm volatile("" ::: "memory");
        a0 = *(LAS v4u*)(L + L_HX + w * 256 + r8 * 16); a1 = *(LAS v4u*)(L + L_HX + w * 256 + 128 + r8 * 16);
      }
      float v[16];
#pragma unroll
      for (int k = 0; k < 8; ++k) {
        v[k] = dot2(a0[3], wp[24 + k], dot2(a0[2], wp[16 + k], dot2(a0[1], wp[8 + k], dot2(a0[0], wp[k], 0.f))));
        v[8 + k] = dot2(a1[3], wp[24 + k], dot2(a1[2], wp[16 + k], dot2(a1[1], wp[8 + k], dot2(a1[0], wp[k], 0.f))));
      }
#pragma unroll
      for (int k = 0; k < 8; ++k) v[k] = tr32(v[k], v[k + 8]);
      f32x4 y;
#pragma unroll
      for (int k = 0; k < 4; ++k) { const float t = tr16(v[k], v[k + 4]); y[k] = t + dpp<0x128>(t); }
      if (n < 8) *(LAS f32x4*)(L + L_RED + w * 512 + (g >> 1) * 256 + n * 32 + (g & 1) * 16) = y;
      asm volatile("s_waitcnt lgkmcnt(0)" ::: "memory");
      unsigned old = 0;
      if (lane == 0) old = __hip_atomic_fetch_add((LAS unsigned*)(L + L_JCNT), 1u, __ATOMIC_RELAXED, __HIP_MEMORY_SCOPE_WORKGROUP);
      if (__builtin_amdgcn_readfirstlane(old) == (unsigned)(nact - 1)) {
        asm volatile("" : "+v"(ep) :: "memory");
        const unsigned tag = ep + 1u;
        f32x2 s = {0.f, 0.f};
#pragma unroll
        for (int ww = 0; ww < 8 / FH; ++ww) s += *(LAS f32x2*)(L + L_RED + ww * 512 + lane * 8);
        if (isH) {
          GAS u64* o = (GAS u64*)(p.ws + W_HG + (hl & 7) * (2048 * FH) + ((hl >> 3) * FH + hf) * 1024 + (lane & 31) * 32 + (lane >> 5) * 8);
          __hip_atomic_store(o, ((u64)tag << 32) | __float_as_uint(s[0]), RLX_AGENT);
          __hip_atomic_store(o + 2, ((u64)tag << 32) | __float_as_uint(s[1]), RLX_AGENT);
        } else {
#pragma unroll
          for (int ww = 8 / FH; ww < 8; ++ww) s += *(LAS f32x2*)(L + L_RED + ww * 512 + lane * 8);
          const int o = (isQ ? W_QG + (lane >> 5) * 16384 + (bid - Q0) * 256 : W_MG + (lane >> 5) * 8192 + (bid - M0) * 256) + (lane & 31) * 8;
          __hip_atomic_store((GAS u64*)(p.ws + o), ((u64)tag << 32) | cvtpk(s[0], s[1]), RLX_AGENT);
        }
        *(volatile LAS unsigned*)(L + L_JCNT) = 9u;
        STAMP(3);
      }
    }
    asm volatile("" : "+v"(ep));
    const unsigned tag = ep + 1u;
    if (lane == 0) __hip_atomic_fetch_add((LAS unsigned*)(L + L_ECNT), 1u, __ATOMIC_RELAXED, __HIP_MEMORY_SCOPE_WORKGROUP);
    if (w == 7) {
      if (isQ) lds_wait<2>(L, L_JCNT, 9); else lds_wait<4>(L, L_FULL, 8 * QTHR);
      v4u t0, t1;
#pragma unroll 1
      for (unsigned spins = 0;;) {
        if (QSTAG) __builtin_amdgcn_s_sleep(QSTAG);
        PSTAMP(10);
        t0 = LD128(rw, lane * 16, W_QG + b * 16384 + kvh * 2048, AUX_SC1); t1 = LD128(rw, lane * 16 + 1024, W_QG + b * 16384 + kvh * 2048, AUX_SC1);
        if (__all((t0[1] == tag) & (t0[3] == tag) & (t1[1] == tag) & (t1[3] == tag)) || ++spins > SPIN_G) break;
        asm volatile("" ::: "memory");
      }
      *(LAS v2u*)(L + L_Q + lane * 8) = v2u{t0[0], t0[2]}; *(LAS v2u*)(L + L_Q + 512 + lane * 8) = v2u{t1[0], t1[2]};
      asm volatile("s_waitcnt lgkmcnt(0)" ::: "memory");
      *(volatile LAS unsigned*)(L + L_QRDY) = 1u;
      STAMP(4);
    }
    v4u wo_r[4], w2_r[2]; v2u w1_r[4];
    {
      const rsrc_t ro = mkrs(p.wo, (ABL & 1) ? 0 : 4096 * 512 * 2), r1 = mkrs(p.w1, hasC && !(ABL & 2) ? 512 * 1792 * 2 : 0), r2 = mkrs(p.w2, hasC && !(ABL & 4) ? 1792 * 512 * 2 : 0);
#define WT(i) (8 * WBASE + (WORD ? (10 * w + (i)) : (8 * (i) + w)) * WSP)
#pragma unroll
      for (int i = 0; i < 4; ++i) { lds_wait<8>(L, L_FULL, WT(i)); wo_r[i] = LD128(ro, (4 * n + i) * 1024 + g * 16, (kvh * 512 + w * 64) * 1024 + cblk * 64, AUX_W); }
#pragma unroll
      for (int i = 0; i < 4; ++i) { lds_wait<8>(L, L_FULL, WT(4 + i)); w1_r[i] = LD64(r1, (4 * n + i) * 3584 + g * 8, w * 64 * 3584 + fblk * 32, AUX_W); }
#pragma unroll
      for (int i = 0; i < 2; ++i) { lds_wait<8>(L, L_FULL, WT(8 + i)); w2_r[i] = LD128(r2, (2 * r8 + i) * 1024 + c8 * 16, fblk * 16384 + w * 128, AUX_W); }
    }
    const v2u xin = LD64(mkrs(p.x, 2048), n * 8, b * 1024 + w * 128, 0);
    v4u mg[4];
#pragma unroll 1
    for (unsigned spins = 0;;) {
      bool ok = true;
#pragma unroll
      for (int k = 0; k < 3; ++k) mg[k] = LD128(rw, n * 16 + k * 2048, W_MG + b * 8192 + w * 256, AUX_SC1);
      mg[3] = LD128(rw, 0, W_MG + b * 8192 + 6144 + ocol * 4, AUX_SC1);
      const v2u e = LD64(rw, (lane & 31) * 8, W_CL + w * 256, AUX_SC1);
#pragma unroll
      for (int k = 0; k < 4; ++k) ok &= (mg[k][1] == tag) & (mg[k][3] == tag);
      ok &= (e[0] == (tag ^ 0x5bd1e995u)) & (e[1] == tag);
      if (__all(ok) || ++spins > SPIN_G) break;
      __builtin_amdgcn_s_sleep(32); asm volatile("" ::: "memory");
    }
    unsigned wo_p[16], w1_p[8], w2_p[8];
#pragma unroll
    for (int pp = 0; pp < 2; ++pp) {
#pragma unroll
      for (int d = 0; d < 4; ++d) { wo_p[pp * 8 + 2 * d] = pk_lo(wo_r[2 * pp][d], wo_r[2 * pp + 1][d]); wo_p[pp * 8 + 2 * d + 1] = pk_hi(wo_r[2 * pp][d], wo_r[2 * pp + 1][d]); }
#pragma unroll
      for (int d = 0; d < 2; ++d) { w1_p[pp * 4 + 2 * d] = pk_lo(w1_r[2 * pp][d], w1_r[2 * pp + 1][d]); w1_p[pp * 4 + 2 * d + 1] = pk_hi(w1_r[2 * pp][d], w1_r[2 * pp + 1][d]); }
    }
#pragma unroll
    for (int d = 0; d < 4; ++d) { w2_p[2 * d] = pk_lo(w2_r[0][d], w2_r[1][d]); w2_p[2 * d + 1] = pk_hi(w2_r[0][d], w2_r[1][d]); }
    f32x4 xf = {lo2f(xin[0]), hi2f(xin[0]), lo2f(xin[1]), hi2f(xin[1])}, ga = {lo2f(mg[0][0]), hi2f(mg[0][0]), lo2f(mg[0][2]), hi2f(mg[0][2])};
    f32x4 sb = {lo2f(mg[1][0]), hi2f(mg[1][0]), lo2f(mg[1][2]), hi2f(mg[1][2])};
    f32x4 cb1 = {rbf(1.f + lo2f(mg[2][0])), rbf(1.f + hi2f(mg[2][0])), rbf(1.f + lo2f(mg[2][2])), rbf(1.f + hi2f(mg[2][2]))};
    const unsigned gw = (g & 2) ? mg[3][2] : mg[3][0];
    float gv = (g & 1) ? hi2f(gw) : lo2f(gw);
#pragma unroll
    for (int k = 0; k < 16; k += 8) asm volatile("" : "+v"(wo_p[k]), "+v"(wo_p[k + 1]), "+v"(wo_p[k + 2]), "+v"(wo_p[k + 3]), "+v"(wo_p[k + 4]), "+v"(wo_p[k + 5]), "+v"(wo_p[k + 6]), "+v"(wo_p[k + 7]));
    asm volatile("" : "+v"(w1_p[0]), "+v"(w1_p[1]), "+v"(w1_p[2]), "+v"(w1_p[3]), "+v"(w1_p[4]), "+v"(w1_p[5]), "+v"(w1_p[6]), "+v"(w1_p[7]));
    asm volatile("" : "+v"(w2_p[0]), "+v"(w2_p[1]), "+v"(w2_p[2]), "+v"(w2_p[3]), "+v"(w2_p[4]), "+v"(w2_p[5]), "+v"(w2_p[6]), "+v"(w2_p[7]));
    asm volatile("" : "+v"(xf), "+v"(ga), "+v"(sb), "+v"(cb1), "+v"(gv));

    int passv = (DRYRUN && w == 0) ? 0 : 1;
    for (;;) {
      asm volatile("" : "+v"(passv));
      const int pass = __builtin_amdgcn_readfirstlane(passv);
      const rsrc_t rt = mkrs(p.ws, pass ? W_END : 0);
      if (pass) { STAMP(5); bar(); STAMP(12); }
      {
        v4u o4[4]; v2u ml[4];
        const int po = W_PO + (bid & ~15) * 2048 + w * 256, mo = W_ML + (bid & ~15) * 128 + (w >> 1) * 16;
#pragma unroll
        for (int i = 0; i < 4; ++i) { o4[i] = LD128(rt, (4 * i + g) * 2048 + n * 16, po, AUX_SC1); ml[i] = LD64(rt, (4 * i + g) * 128, mo, AUX_SC1); }
#pragma unroll 1
        for (unsigned spins = 0;;) {
          bool bad[4];
#pragma unroll
          for (int i = 0; i < 4; ++i) bad[i] = max(umax4(o4[i]), max(ml[i][0], ml[i][1])) == SENT;
          if (!__any(bad[0] | bad[1] | bad[2] | bad[3]) || ++spins > SPIN_G) break;
          if (POLL_SLEEP) __builtin_amdgcn_s_sleep(POLL_SLEEP);
          asm volatile("" ::: "memory");
#pragma unroll
          for (int i = 0; i < 4; ++i) if (bad[i]) { o4[i] = LD128(rt, (4 * i + g) * 2048 + n * 16, po, AUX_SC1); ml[i] = LD64(rt, (4 * i + g) * 128, mo, AUX_SC1); }
        }
        if (pass) STAMP(6);
        float mi[4], M;
#pragma unroll
        for (int i = 0; i < 4; ++i) mi[i] = __uint_as_float(ml[i][0]);
        M = xg_max(fmaxf(fmaxf(mi[0], mi[1]), fmaxf(mi[2], mi[3])));
        float Ls = 0.f; f32x4 o = {0.f, 0.f, 0.f, 0.f};
#pragma unroll
        for (int i = 0; i < 4; ++i) { const float e = __builtin_amdgcn_exp2f((mi[i] - M) * L2E); Ls += e * __uint_as_float(ml[i][1]); o += e * __builtin_bit_cast(f32x4, o4[i]); }
        const float inv = rcp(xg_sum(Ls));
        const unsigned p0 = cvtpk(xg_sum(o[0]) * inv, xg_sum(o[1]) * inv), p1 = cvtpk(xg_sum(o[2]) * inv, xg_sum(o[3]) * inv);
        float y[8];
#pragma unroll
        for (int k = 0; k < 8; ++k) y[k] = row_sum(dot2(p1, wo_p[8 + k], dot2(p0, wo_p[k], 0.f)));
        if (n < 2) *(LAS f32x4*)(L + L_RAB + w * 128 + g * 32 + n * 16) = n ? f32x4{y[4], y[5], y[6], y[7]} : f32x4{y[0], y[1], y[2], y[3]};
        if (pass) { STAMP(13); bar(); }
        if (w == 0) {
          f32x4 s = *(LAS f32x4*)(L + L_RAB + r8 * 128 + c8 * 16);
#pragma unroll
          for (int k = 0; k < 4; ++k) { float t = s[k]; t += dpp<0x128>(t); s[k] = xg_sum(t); }
          const v4u su = __builtin_bit_cast(v4u, s);
          ST128((v4u{nosent(su[0]), nosent(su[1]), nosent(su[2]), nosent(su[3])}), rt, lane < 8 ? lane * 16 : (int)OOB, W_AB + ((b * 8 + kvh) * 512 + cblk * 32) * 4, AUX_SC1);
          if (pass) STAMP(7);
        }
      }
      {
        v4u a[2];
#pragma unroll
        for (int i = 0; i < 2; ++i) a[i] = LD128(rt, (4 * i + g) * 2048 + n * 16, W_AB + b * 16384 + w * 256, AUX_SC1);
#pragma unroll 1
        for (unsigned spins = 0;;) {
          const bool bad0 = umax4(a[0]) == SENT, bad1 = umax4(a[1]) == SENT;
          if (!__any(bad0 | bad1) || ++spins > SPIN_G) break;
          if (POLL_SLEEP) __builtin_amdgcn_s_sleep(POLL_SLEEP);
          asm volatile("" ::: "memory");
          if (bad0) a[0] = LD128(rt, g * 2048 + n * 16, W_AB + b * 16384 + w * 256, AUX_SC1);
          if (bad1) a[1] = LD128(rt, (4 + g) * 2048 + n * 16, W_AB + b * 16384 + w * 256, AUX_SC1);
        }
        if (pass) STAMP(8);
        const f32x4 as = __builtin_bit_cast(f32x4, a[0]) + __builtin_bit_cast(f32x4, a[1]);
        f32x4 x1;
#pragma unroll
        for (int k = 0; k < 4; ++k) x1[k] = rbf(xf[k] + rbf(ga[k] * rbf(xg_sum(as[k]))));
        const float pv = __builtin_bit_cast(float, __builtin_amdgcn_readfirstlane(__builtin_bit_cast(int, x1[0])));
        const f32x4 dx = x1 - pv;
        const float s1 = row_sum((dx[0] + dx[1]) + (dx[2] + dx[3])), s2 = row_sum((dx[0] * dx[0] + dx[1] * dx[1]) + (dx[2] * dx[2] + dx[3] * dx[3]));
        if (lane == 0) *(LAS f32x2*)(L + L_ST + w * 8) = f32x2{pv + s1 * (1.f / 64.f), s2 - s1 * s1 * (1.f / 64.f)};
        if (w == xl && lane == j16) *(LAS f32x4*)(L + L_X1) = x1;
        if (pass) { STAMP(14); bar(); }
        f32x4 st[4];
#pragma unroll
        for (int k = 0; k < 4; ++k) st[k] = *(LAS f32x4*)(L + L_ST + k * 16);
        const float mu = (((st[0][0] + st[0][2]) + (st[1][0] + st[1][2])) + ((st[2][0] + st[2][2]) + (st[3][0] + st[3][2]))) * 0.125f;
        float m2 = 0.f, dv = 0.f;
#pragma unroll
        for (int k = 0; k < 4; ++k) { m2 += st[k][1] + st[k][3]; const float t0 = st[k][0] - mu, t1 = st[k][2] - mu; dv += t0 * t0 + t1 * t1; }
        const float mean = rbf(mu), var = rbf(fmaxf(m2 + 64.f * dv, 0.f) * (1.f / 512.f));
        const float isd = rcp(rbf(__builtin_amdgcn_sqrtf(rbf(var + 1e-5f))));
        float h[4];
#pragma unroll
        for (int k = 0; k < 4; ++k) h[k] = rbf(rbf(rbf(x1[k] - mean) * isd) * cb1[k]) + sb[k];
        const unsigned p0 = cvtpk(h[0], h[1]), p1 = cvtpk(h[2], h[3]);
        f32x4 y;
#pragma unroll
        for (int k = 0; k < 4; ++k) y[k] = row_sum(dot2(p1, w1_p[4 + k], dot2(p0, w1_p[k], 0.f)));
        if (n == 0) *(LAS f32x4*)(L + L_RC + w * 64 + g * 16) = y;
        if (pass) { STAMP(15); bar(); }
        const f32x2 s = *(LAS f32x2*)(L + L_RC + c8 * 64 + r8 * 8);
        float t0 = s[0], t1 = s[1];
        t0 += dpp<0xB1>(t0); t1 += dpp<0xB1>(t1); t0 += dpp<0x4E>(t0); t1 += dpp<0x4E>(t1); t0 += dpp<0x141>(t0); t1 += dpp<0x141>(t1);
        const unsigned d = cvtpk(silu_f(rbf(t0)), silu_f(rbf(t1)));
        float z[8];
#pragma unroll
        for (int k = 0; k < 8; ++k) z[k] = dot2(d, w2_p[k], 0.f);
        v4u o;
#pragma unroll
        for (int k = 0; k < 4; ++k) { float t = tr32(z[k], z[k + 4]); t = tr16(t, t); t += dpp<0x128>(t); o[k] = nosent(__float_as_uint(t)); }
        ST128(o, rt, (lane & 24) == 0 && hasC ? (c8 >> 2) * (112 * 128) + (c8 & 3) * 32 + (lane >> 5) * 16 : (int)OOB, W_OP + ((b * 16 + 2 * w) * 112 + fblk) * 128, AUX_SC1);
        if (pass) TSTAMP(9);
      }
      if (w == 0) {
        const int oo = W_OP + (b * 16 + (ocol >> 5)) * (112 * 128) + (ocol & 31) * 4;
        const int o0 = lane < 56 ? lane * 128 : (int)OOB, o1 = lane < 56 ? (lane + 56) * 128 : (int)OOB;
        const float xv = *(LAS float*)(L + L_X1 + g * 4);
        v4u a0 = LD128(rt, o0, oo, AUX_SC1), a1 = LD128(rt, o1, oo, AUX_SC1);
#pragma unroll 1
        for (unsigned spins = 0;;) {
          const bool bad0 = umax4(a0) == SENT, bad1 = umax4(a1) == SENT;
          if (!__any(bad0 | bad1) || ++spins > SPIN_G) break;
          if (POLL_SLEEP) __builtin_amdgcn_s_sleep(POLL_SLEEP);
          asm volatile("" ::: "memory");
          if (bad0) a0 = LD128(rt, o0, oo, AUX_SC1);
          if (bad1) a1 = LD128(rt, o1, oo, AUX_SC1);
        }
        if (pass) TSTAMP(10);
        const f32x4 y = __builtin_bit_cast(f32x4, a0) + __builtin_bit_cast(f32x4, a1);
        const float y2 = row_sum(tr16(tr32(y[0], y[2]), tr32(y[1], y[3])));
        const unsigned r = cvtpk(xv + rbf(gv * rbf(y2)), 0.f);
        if (pass && n == 0) *(GAS u16*)(p.out + b * 512 + ocol + g) = (u16)r;
        __builtin_amdgcn_raw_buffer_store_b32(tag, rt, (lane == 0 && bid == 0) ? 0 : (int)OOB, W_EPOCH, AUX_SC1);
        if (pass) TSTAMP(11);
      }
      if (pass) break;
      passv = 1;
    }
    if (w == 0) { const u64 ts = *(volatile LAS u64*)(L + L_STAMP), tx = *(volatile LAS u64*)(L + L_STAMP + PROBEW * 128 + PROBEK * 8);
      const u64 tgt = ts + 7000 + (tx ? PROBEM * (tx - ts) : 0); unsigned gd = 0; while (__builtin_amdgcn_s_memrealtime() < tgt && ++gd < 2000000u) __builtin_amdgcn_s_sleep(1); }
    STAMP_OUT();
    return;
  }

  {
    const rsrc_t rk = mkrs(p.K + (size_t)bid * (NUNIT * 4096), NUNIT * 8192), rv = mkrs(p.V + (size_t)bid * (NUNIT * 4096), NUNIT * 8192);
    const int k0 = g * 256 + ((n ^ g) * 16), k1 = k0 ^ 64, k2 = k0 ^ 128, k3 = k0 ^ 192, ve = g * 256 + ((n ^ (2 * g)) * 16), vo = ve ^ 128;
    unsigned freed = 0; int slot = 0, pub = 0;
    const int deep = (QHOLD && isQ) ? QCATCH : 0;
    STAMP(3);
#define PACE(done) do { if (u < NSLOW) { waitvm<DSLOW - 4>(); pub = max(pub, (done) - (DSLOW - 4)); } else if (u < deep) { waitvm<DDEEP - 4>(); pub = max(pub, (done) - (DDEEP - 4)); } \
                        else { waitvm<DEPTH - 4>(); pub = max(pub, (done) - (DEPTH - 4)); } *FULL = pub; } while (0)
#pragma unroll 1
    for (int u = 0; u < NUNIT; ++u) {
      if (QHOLD && u == QHOLD && isQ) { waitvm<0>(); pub = 16 * u; *FULL = pub; lds_wait<1>(L, L_JCNT, 9); }
      if (LPRIO && u == 1) __builtin_amdgcn_s_setprio(LPRIO);
      if (u >= NSLOT && freed < (unsigned)(u - NSLOT + 1)) {
        unsigned spins = 0; bool drained = false;
        for (;;) {
          const v4u f = *(volatile LAS v4u*)(L + L_FREE);
          freed = __builtin_amdgcn_readfirstlane(min(min(f[0], f[1]), min(f[2], f[3])));
          if (freed >= (unsigned)(u - NSLOT + 1)) break;
          if (++spins > SPIN_L) { freed = SENT; break; }
          if (!drained) { if (u == NSLOT) STAMP(4); waitvm<0>(); pub = 16 * u; *FULL = pub; drained = true; STAMP(2); }
          __builtin_amdgcn_s_sleep(1);
        }
      }
      const unsigned lb = slot * 16384;
      dma4(rk, k0, k1, k2, k3, u * 8192, lb); PACE(16 * u + 4);
      dma4(rk, k0, k1, k2, k3, u * 8192 + 4096, lb + 4096); PACE(16 * u + 8);
      dma4(rv, ve, vo, ve, vo, u * 8192, lb + 8192); PACE(16 * u + 12);
      dma4(rv, ve, vo, ve, vo, u * 8192 + 4096, lb + 12288); PACE(16 * u + 16);
      slot = (slot + 1 == NSLOT) ? 0 : slot + 1;
    }
    if (DEPTH > 28) { waitvm<24>(); *FULL = 16 * NUNIT - 24; }
    if (DEPTH > 20) { waitvm<16>(); *FULL = 16 * NUNIT - 16; }
    waitvm<8>(); *FULL = 16 * NUNIT - 8;
    waitvm<0>(); *FULL = 16 * NUNIT;
    STAMP(1);
    STAMP_OUT();
  }
}

extern "C" void kernel_launch(void* const* d_in, const int* in_sizes, int n_in, void* d_out, int out_size, void* d_ws, size_t ws_size, hipStream_t stream) {
  Params p;
  p.x = (const u16*)d_in[0]; p.K = (const u16*)d_in[1]; p.V = (const u16*)d_in[2]; p.wq = (const u16*)d_in[3]; p.wo = (const u16*)d_in[4];
  p.w1 = (const u16*)d_in[6]; p.w2 = (const u16*)d_in[7]; p.c = (const u16*)d_in[8]; p.wmod = (const u16*)d_in[9];
  p.out = (u16*)d_out; p.ws = (unsigned char*)d_ws;
  (void)hipFuncSetAttribute((const void*)adaln_block_kernel, hipFuncAttributeMaxDynamicSharedMemorySize, L_END);
  hipLaunchKernelGGL(adaln_block_kernel, dim3(NWG), dim3(NTHR), L_END, stream, p);
}
```

```cpp
#include <hip/hip_runtime.h>
#include <hip/hip_bf16.h>
#include <stdint.h>

typedef unsigned short u16;
typedef unsigned long long u64;
typedef unsigned v4u __attribute__((ext_vector_type(4)));
typedef unsigned v2u __attribute__((ext_vector_type(2)));
typedef float f32x4 __attribute__((ext_vector_type(4)));
typedef float f32x2 __attribute__((ext_vector_type(2)));
typedef short bf16x8 __attribute__((ext_vector_type(8)));
typedef short s16x4 __attribute__((ext_vector_type(4)));
typedef __bf16 bf16x2_t __attribute__((ext_vector_type(2)));
typedef __amdgpu_buffer_rsrc_t rsrc_t;
#define GAS __attribute__((address_space(1)))
#define LAS __attribute__((address_space(3)))
#define RLX_AGENT __ATOMIC_RELAXED, __HIP_MEMORY_SCOPE_AGENT

#define NSLOT 9
#define DEPTH 22
#define DRYRUN 1
#define FH 2
#define NSLOW 0
#define DSLOW 18
#define DDEEP 32
#define QHOLD 2
#define QCATCH 24
#define CODEWARM 1
#define AUX_W 0
#define CPRE 12
#define DPRE 12
#define NPRE 2
#define HT 280
#define TAU 0
#define TA0 60
#define EHI 30
#define ELO 30
#define MLONLY 2
#define POLL_SLEEP2 0
#define EARLY 0
#define MCATCH 12
#define QT 520
#define NQP 1
#define CDRY 1
#define CLRW 0
#define CLT 24
#define NOCLR 0
#define ABL 0
#define WSP 9
#define WORD 1
#define WBASE 20
#define LPRIO 2
#define POLL_SLEEP 0
#define HSTAG 40
#define QTHR 9
#define QSTAG 30

constexpr int NWG = 256, NWAVE = 13, NTHR = NWAVE * 64, NUNIT = 64;
constexpr int NH = 16 * FH, Q0 = NH, M0 = Q0 + 64, JEND = M0 + 32;
constexpr unsigned SENT = 0xFFFFFFFFu, OOB = 0x7FFFFFF0u;
constexpr int AUX_NT = 2, AUX_SC1 = 16;
constexpr int W_EPOCH = 0, W_CL = 0x800, W_QG = 0x2000  , W_MG = 0xA000  , W_AB = 0x10000, W_ML = 0x18000,
              W_PO = 0x20000, W_OP = 0xA0000  , W_HG = 0x110000  , W_END = 0x120000;
constexpr int W_STAMP = 0x200000;
constexpr int L_CTRL = NSLOT * 16384, L_FULL = L_CTRL, L_QRDY = L_CTRL + 4, L_JCNT = L_CTRL + 8, L_ECNT = L_CTRL + 12, L_FREE = L_CTRL + 16, L_CLRD = L_CTRL + 32, L_Q = L_CTRL + 64,
              L_RED = L_Q + 1024  , L_HX = L_RED + 4096  , L_RAB = L_HX + 2048, L_ST = L_RAB + 1024, L_RC = L_ST + 128, L_X1 = L_RC + 512,
              L_STAMP = L_X1 + 16, L_END = L_STAMP + NWAVE * 128;
static_assert(L_END <= 160 * 1024, "LDS");
constexpr unsigned SPIN_G = 1u << 17, SPIN_L = 1u << 22;

#define STAMP(k) do {} while (0)
#define TSTAMP(k) do {} while (0)
#define PSTAMP(k) do {} while (0)
#define STAMP_INIT() do {} while (0)
#define STAMP_OUT() do {} while (0)

__device__ __forceinline__ float lo2f(unsigned u) { return __builtin_bit_cast(float, u << 16); }
__device__ __forceinline__ float hi2f(unsigned u) { return __builtin_bit_cast(float, u & 0xffff0000u); }
__device__ __forceinline__ unsigned cvtpk(float lo, float hi) { f32x2 v = {lo, hi}; return __builtin_bit_cast(unsigned, __builtin_convertvector(v, bf16x2_t)); }
__device__ __forceinline__ float rbf(float x) { return lo2f(cvtpk(x, x)); }
__device__ __forceinline__ float rcp(float x) { return __builtin_amdgcn_rcpf(x); }
__device__ __forceinline__ float silu_f(float x) { return x * rcp(1.f + __builtin_amdgcn_exp2f(x * -1.4426950408889634f)); }
__device__ __forceinline__ f32x4 mfma(v4u a, v4u b, f32x4 c) {
  return __builtin_amdgcn_mfma_f32_16x16x32_bf16(__builtin_bit_cast(bf16x8, a), __builtin_bit_cast(bf16x8, b), c, 0, 0, 0);
}
__device__ __forceinline__ float dot2(unsigned a, unsigned b, float c) {
  return __builtin_amdgcn_fdot2_f32_bf16(__builtin_bit_cast(bf16x2_t, a), __builtin_bit_cast(bf16x2_t, b), c, false);
}
__device__ __forceinline__ unsigned pk_lo(unsigned r0, unsigned r1) { return __builtin_amdgcn_perm(r1, r0, 0x05040100u); }
__device__ __forceinline__ unsigned pk_hi(unsigned r0, unsigned r1) { return __builtin_amdgcn_perm(r1, r0, 0x07060302u); }
__device__ __forceinline__ unsigned nosent(unsigned d) { return d < 0xFFFFFFFEu ? d : 0xFFFFFFFEu; }
template <int CTRL> __device__ __forceinline__ float dpp(float x) {
  return __builtin_bit_cast(float, __builtin_amdgcn_mov_dpp(__builtin_bit_cast(int, x), CTRL, 0xf, 0xf, true));
}
__device__ __forceinline__ float row_sum(float x) { x += dpp<0x128>(x); x += dpp<0x124>(x); x += dpp<0x122>(x); x += dpp<0x121>(x); return x; }
__device__ __forceinline__ float tr32(float a, float b) {
  auto s = __builtin_amdgcn_permlane32_swap(__float_as_uint(a), __float_as_uint(b), false, false);
  return __uint_as_float(s[0]) + __uint_as_float(s[1]);
}
__device__ __forceinline__ float tr16(float a, float b) {
  auto s = __builtin_amdgcn_permlane16_swap(__float_as_uint(a), __float_as_uint(b), false, false);
  return __uint_as_float(s[0]) + __uint_as_float(s[1]);
}
__device__ __forceinline__ float xg_sum(float x) { x = tr16(x, x); return tr32(x, x); }
__device__ __forceinline__ float xg_max(float x) {
  auto s = __builtin_amdgcn_permlane16_swap(__float_as_uint(x), __float_as_uint(x), false, false);
  x = fmaxf(__uint_as_float(s[0]), __uint_as_float(s[1]));
  auto t = __builtin_amdgcn_permlane32_swap(__float_as_uint(x), __float_as_uint(x), false, false);
  return fmaxf(__uint_as_float(t[0]), __uint_as_float(t[1]));
}
__device__ __forceinline__ float wave_sum(float x) { return xg_sum(row_sum(x)); }
__device__ __forceinline__ unsigned umax4(v4u v) { return max(max(v[0], v[1]), max(v[2], v[3])); }
template <int N> __device__ __forceinline__ void waitvm() { asm volatile("s_waitcnt vmcnt(%0)" :: "n"(N) : "memory"); }
__device__ __forceinline__ void bar() { asm volatile("s_waitcnt lgkmcnt(0)\n\ts_barrier" ::: "memory"); }
__device__ __forceinline__ rsrc_t mkrs(const void* p, unsigned bytes) { return __builtin_amdgcn_make_buffer_rsrc((void*)p, 0, bytes, 0x00020000); }
__device__ __forceinline__ void dma4(rsrc_t rs, int v0, int v1, int v2, int v3, int soff, unsigned lds) {
  asm volatile("s_mov_b32 m0, %6\n\ts_nop 0\n\tbuffer_load_dwordx4 %0, %4, %5 offen nt lds\n\tbuffer_load_dwordx4 %1, %4, %5 offen offset:1024 nt lds\n\t"
               "buffer_load_dwordx4 %2, %4, %5 offen offset:2048 nt lds\n\tbuffer_load_dwordx4 %3, %4, %5 offen offset:3072 nt lds"
               :: "v"(v0), "v"(v1), "v"(v2), "v"(v3), "s"(rs), "s"(soff), "s"(lds) : "memory");
}
#define LD128(rs, vo, so, aux) __builtin_amdgcn_raw_buffer_load_b128(rs, vo, so, aux)
#define LD64(rs, vo, so, aux) __builtin_amdgcn_raw_buffer_load_b64(rs, vo, so, aux)
#define LD32(rs, vo, so, aux) __builtin_amdgcn_raw_buffer_load_b32(rs, vo, so, aux)
#define ST128(v, rs, vo, so, aux) __builtin_amdgcn_raw_buffer_store_b128(v, rs, vo, so, aux)
#define ST64(v, rs, vo, so, aux) __builtin_amdgcn_raw_buffer_store_b64(v, rs, vo, so, aux)

template <int CTRL> __device__ __forceinline__ unsigned dppu(unsigned x) { return (unsigned)__builtin_amdgcn_mov_dpp((int)x, CTRL, 0xf, 0xf, true); }
typedef short bf16x4 __attribute__((ext_vector_type(4)));
__device__ __forceinline__ f32x4 mfma16(v2u a, v2u b) {
  return __builtin_amdgcn_mfma_f32_16x16x16bf16_1k(__builtin_bit_cast(bf16x4, a), __builtin_bit_cast(bf16x4, b), f32x4{0.f, 0.f, 0.f, 0.f}, 0, 0, 0);
}
__device__ __forceinline__ void afrag(float x, v4u& a0, v4u& a1) {
  const unsigned P = cvtpk(x, dpp<0x101>(x));
  a0 = v4u{P, dppu<0x102>(P), dppu<0x104>(P), dppu<0x106>(P)};
  a1 = v4u{dppu<0x108>(P), dppu<0x10A>(P), dppu<0x10C>(P), dppu<0x10E>(P)};
}
__device__ __forceinline__ float tr4(f32x4 v) { return tr16(tr32(v[0], v[2]), tr32(v[1], v[3])); }
struct Params { const u16 *x, *K, *V, *wq, *wo, *w1, *w2, *c, *wmod; u16* out; unsigned char* ws; };

__device__ __forceinline__ unsigned lds_get(LAS unsigned char* L, int off) { return __builtin_amdgcn_readfirstlane(*(volatile LAS unsigned*)(L + off)); }
template <int SL> __device__ __forceinline__ void lds_wait(LAS unsigned char* L, int off, unsigned want) {
#pragma unroll 1
  for (unsigned spins = 0; lds_get(L, off) < want && ++spins < SPIN_L;) __builtin_amdgcn_s_sleep(SL);
  asm volatile("" ::: "memory");
}

__global__ void __launch_bounds__(NTHR, 4) __attribute__((aligned(4096))) adaln_block_kernel(Params p) {
  extern __shared__ __attribute__((aligned(16))) unsigned char smem[];
  LAS unsigned char* L = (LAS unsigned char*)smem;
  const int tid = threadIdx.x, lane = tid & 63, w = __builtin_amdgcn_readfirstlane(tid >> 6);
  const int bid = blockIdx.x;
  unsigned cw[5] = {0u, 0u, 0u, 0u, 0u};
  if (CODEWARM && w == 9 && (CODEWARM == 2 || (bid >> 4) == 8)) {
    u64 pc0; unsigned clen;
    asm volatile("s_getpc_b64 %0\n.Ladaln_code_begin:\n\ts_mov_b32 %1, .Ladaln_code_end-.Ladaln_code_begin" : "=s"(pc0), "=s"(clen));
    const rsrc_t rc = mkrs((const void*)pc0, clen);
#pragma unroll
    for (int i = 0; i < 5; ++i) cw[i] = LD32(rc, lane * 64, i * 4096, 0);
  }
  STAMP_INIT();
  if (tid < 16) ((LAS unsigned*)(L + L_CTRL))[tid] = 0u;
  asm volatile("s_waitcnt lgkmcnt(0)\n\ts_barrier" : "+s"(p.x), "+s"(p.K), "+s"(p.V), "+s"(p.wq), "+s"(p.wo), "+s"(p.w1), "+s"(p.w2), "+s"(p.c), "+s"(p.wmod), "+s"(p.out), "+s"(p.ws) :: "memory");
  const int xl = bid & 7, jl = bid >> 3, b = bid >> 7, kvh = (bid >> 4) & 7, mem = bid & 15;
  const int g = lane >> 4, n = lane & 15, r8 = lane >> 3, c8 = lane & 7;
  volatile LAS unsigned* FULL = (volatile LAS unsigned*)(L + L_FULL);
  constexpr float L2E = 1.4426950408889634f;
  const bool isH = bid < Q0, isQ = !isH && bid < M0;
  const rsrc_t rw = mkrs(p.ws, W_END);
  const int cblk = 2 * ((mem + kvh) & 7) + (mem >> 3);
  const int j16 = jl & 15;
  const bool hasC = j16 < 14;
  const int fblk = xl * 14 + j16;
  const int ocol = xl * 64 + j16 * 4;

  if (w >= 8 && w < 12) {
  const int c = w - 8;
  asm volatile("" :: "v"(cw[0]), "v"(cw[1]), "v"(cw[2]), "v"(cw[3]), "v"(cw[4]));
  if (CLRW && c == 1) {
    if (CLRW > 1) __builtin_amdgcn_s_sleep(CLRW);
      const v4u s4 = {SENT, SENT, SENT, SENT};
      if (!(NOCLR & 1)) { ST128(s4, rw, lane * 16, W_PO + bid * 2048, AUX_SC1);
      ST128(s4, rw, lane * 16 + 1024, W_PO + bid * 2048, AUX_SC1); }
      const int oo = hasC && !(NOCLR & 2) ? W_OP + (b * 16 * 112 + fblk) * 128 : (int)OOB;
      ST128(s4, rw, r8 * (112 * 128) + c8 * 16, oo, AUX_SC1);
      ST128(s4, rw, (8 + r8) * (112 * 128) + c8 * 16, oo, AUX_SC1);
      if (!(NOCLR & 4)) ST128(s4, rw, lane < 4 ? W_ML + bid * 128 + lane * 16 : lane < 12 ? W_AB + ((b * 8 + kvh) * 512 + cblk * 32) * 4 + (lane - 4) * 16 : (int)OOB, 0, AUX_SC1);
      waitvm<0>();
      *(volatile LAS unsigned*)(L + L_CLRD) = 1u;
  }
  f32x4 acc0, acc1;
  float m, l, mb;
  const int r7 = 4 * (g & 1) + (n >> 2);
  const int va0 = 8192 + (4 * g + (n >> 2)) * 256 + (((4 * c + ((n >> 1) & 1)) ^ (2 * r7)) * 16) + 8 * (n & 1), va1 = va0 ^ 32;
  int po = n < 4 ? W_PO + bid * 2048 + n * 512 + c * 128 + g * 16 : (int)OOB;
  asm volatile("" : "+v"(po));
  const int ka = n * 256 + ((g ^ n) * 16);
  unsigned full, live;
  v4u qf[4];
  auto wait_full = [&](unsigned want) {
    for (unsigned spins = 0; full < want;) { full = __builtin_amdgcn_readfirstlane(*FULL); if (++spins > SPIN_L) full = SENT; }
    asm volatile("" ::: "memory");
  };
  auto read_k = [&](LAS unsigned char* S, v4u (&kf)[8]) {
#pragma unroll
    for (int i = 0; i < 8; ++i) kf[i] = *(LAS v4u*)(S + (ka ^ ((i & 3) * 64)) + (i >> 2) * 4096);
    __builtin_amdgcn_sched_barrier(0);
  };
  auto read_v = [&](LAS unsigned char* S, v4u& v0, v4u& v1, int u) {
    const v2u a00 = __builtin_bit_cast(v2u, __builtin_amdgcn_ds_read_tr16_b64_v4i16((LAS s16x4*)(S + va0)));
    const v2u a01 = __builtin_bit_cast(v2u, __builtin_amdgcn_ds_read_tr16_b64_v4i16((LAS s16x4*)(S + va0 + 4096)));
    const v2u a10 = __builtin_bit_cast(v2u, __builtin_amdgcn_ds_read_tr16_b64_v4i16((LAS s16x4*)(S + va1)));
    const v2u a11 = __builtin_bit_cast(v2u, __builtin_amdgcn_ds_read_tr16_b64_v4i16((LAS s16x4*)(S + va1 + 4096)));
    v0 = v4u{a00[0], a00[1], a01[0], a01[1]}; v1 = v4u{a10[0], a10[1], a11[0], a11[1]};
    asm volatile("s_waitcnt lgkmcnt(0)" ::: "memory");
    *(volatile LAS unsigned*)(L + L_FREE + c * 4) = (unsigned)(u + 1) & live;
  };
  auto probs = [&](const v4u (&kf)[8]) -> v4u {
    constexpr float SC = 0.08838834764831845f;
    f32x4 s0 = {0.f, 0.f, 0.f, 0.f}, s1 = {0.f, 0.f, 0.f, 0.f};
#pragma unroll
    for (int s = 0; s < 4; ++s) { s0 = mfma(kf[s], qf[s], s0); s1 = mfma(kf[4 + s], qf[s], s1); }
    const unsigned u0 = cvtpk(s0[0], s0[1]), u1 = cvtpk(s0[2], s0[3]), u2 = cvtpk(s1[0], s1[1]), u3 = cvtpk(s1[2], s1[3]);
    const unsigned us = (n & 8) ? ((n & 4) ? u3 : u2) : ((n & 4) ? u1 : u0);
    const unsigned ts = cvtpk(lo2f(us) * SC, hi2f(us) * SC);
    const float e0 = lo2f(ts), e1 = hi2f(ts);
    float bm = fmaxf(e0, e1);
    if (__any(bm > m)) {
      bm = fmaxf(bm, dpp<0x124>(bm)); bm = fmaxf(bm, dpp<0x128>(bm));
      const float mn = fmaxf(m, xg_max(bm));
      const float alpha = __builtin_amdgcn_exp2f((m - mn) * L2E);
      m = mn; mb = mn * L2E; acc0 *= alpha; acc1 *= alpha; l *= alpha;
    }
    const float p0 = __builtin_amdgcn_exp2f(e0 * L2E - mb), p1 = __builtin_amdgcn_exp2f(e1 * L2E - mb);
    l += p0 + p1;
    const unsigned pp = cvtpk(p0, p1);
    return v4u{pp, dppu<0x12C>(pp), dppu<0x128>(pp), dppu<0x124>(pp)};
  };
  int passv = (CDRY && c == 0) ? 0 : 1;
  for (;;) {
  asm volatile("" : "+v"(passv));
  const int pass = __builtin_amdgcn_readfirstlane(passv);
  live = pass ? SENT : 0u; full = ~live;
  acc0 = f32x4{0.f, 0.f, 0.f, 0.f}; acc1 = acc0; m = -INFINITY; l = 0.f; mb = 0.f;
  const rsrc_t rt = mkrs(p.ws, W_END & live);
  {
    v4u kp[NPRE + 1][8], vp[NPRE + 1][2];
#pragma unroll
    for (int u = 0; u < NPRE; ++u) {
      lds_wait<2>(L, L_FULL, (16 * u + 16) & live);
      read_k(L + u * 16384, kp[u]); read_v(L + u * 16384, vp[u][0], vp[u][1], u);
    }
    lds_wait<1>(L, L_QRDY, 1 & live);
    STAMP(1);
#pragma unroll
    for (int s = 0; s < 4; ++s) qf[s] = *(LAS v4u*)(L + L_Q + (n & 3) * 256 + s * 64 + g * 16);
#pragma unroll
    for (int u = 0; u < NPRE; ++u) { const v4u pf = probs(kp[u]); acc0 = mfma(vp[u][0], pf, acc0); acc1 = mfma(vp[u][1], pf, acc1); }
  }
  int slot = NPRE;
#pragma unroll 1
  for (int u = pass ? NPRE : NUNIT - 1; u < NUNIT; ++u) {
    LAS unsigned char* S = L + slot * 16384;
    wait_full(16 * u + 8);
    v4u kf[8];
    read_k(S, kf);
    v4u pf = probs(kf);
    if (u == NUNIT - 2) lds_wait<1>(L, L_CLRD, 1);
    if (u == NUNIT - 1 && c == 0) {
      float lt = l + dpp<0x124>(l); lt += dpp<0x128>(lt); lt = xg_sum(lt);
      ST64((v2u{nosent(__float_as_uint(m)), nosent(__float_as_uint(lt))}), rt, lane < 4 ? lane * 16 : (int)OOB, W_ML + bid * 128, AUX_SC1);
      STAMP(3);
    }
    asm volatile("" : "+v"(pf), "+v"(acc0), "+v"(acc1), "+v"(l));
    wait_full(16 * u + 16);
    v4u v0, v1;
    read_v(S, v0, v1, u);
    acc0 = mfma(v0, pf, acc0); acc1 = mfma(v1, pf, acc1);
    slot = (slot + 1 == NSLOT) ? 0 : slot + 1;
  }
  {
    const v4u v0 = __builtin_bit_cast(v4u, acc0), v1 = __builtin_bit_cast(v4u, acc1);
    ST128((v4u{nosent(v0[0]), nosent(v0[1]), nosent(v0[2]), nosent(v0[3])}), rt, po, 0, AUX_SC1);
    ST128((v4u{nosent(v1[0]), nosent(v1[1]), nosent(v1[2]), nosent(v1[3])}), rt, po, 64, AUX_SC1);
  }
  if (pass) break;
  passv = 1;
  }
  STAMP(5);
  bar();
  asm volatile(".Ladaln_code_end:");
  STAMP_OUT();
  return;
  }

  if (w < 8) {
    unsigned ep = LD32(rw, 0, W_EPOCH, AUX_SC1);
    const unsigned tstart = (unsigned)__builtin_amdgcn_s_memrealtime();
    const int nact = isH ? 8 / FH : 8;
    if (bid < JEND && w < nact) {
      const int hl = bid & 15, hf = bid >> 4;
      const int pitch = isQ ? 8192 : 6144;
      const int cb = (isH ? hl : isQ ? bid - Q0 : bid - M0 + 16) * 128;
      const int row0 = (isH ? hf * (512 / FH) : 0) + w * 64;
      const rsrc_t rj = mkrs(isQ ? p.wq : p.wmod, 512 * pitch), ri = mkrs(isQ ? p.x : p.c, 2048);
      const int io = isQ ? lane * 16 : (n & 1) * 1024 + row0 * 2 + g * 16;
      const v4u i0 = LD128(ri, io, 0, 0), i1 = LD128(ri, io, isQ ? 1024 : 64, 0);
      unsigned xd0 = __builtin_amdgcn_raw_buffer_load_b16(ri, lane * 2, w * 128, 0), xd1 = __builtin_amdgcn_raw_buffer_load_b16(ri, lane * 2, 1024 + w * 128, 0);
      v2u wr[16];
#pragma unroll
      for (int i = 0; i < 16; ++i) wr[i] = LD64(rj, g * 8 * pitch + n * 8, (row0 + (i >> 3) * 32 + (i & 7)) * pitch + cb, AUX_NT);
      __builtin_amdgcn_sched_barrier(0);
      STAMP(9);
      v4u a0, a1;
      float ln0 = 0.f, ln1 = 0.f;
      if (!isQ) {
#pragma unroll
        for (int k = 0; k < 4; ++k) { a0[k] = cvtpk(silu_f(lo2f(i0[k])), silu_f(hi2f(i0[k]))); a1[k] = cvtpk(silu_f(lo2f(i1[k])), silu_f(hi2f(i1[k]))); }
      } else {
        a0 = i0; a1 = i1;
        asm volatile("" : "+v"(xd0), "+v"(xd1));
        float sm0 = 0.f, sm1 = 0.f;
#pragma unroll
        for (int k = 0; k < 4; ++k) { sm0 += lo2f(i0[k]) + hi2f(i0[k]); sm1 += lo2f(i1[k]) + hi2f(i1[k]); }
        const float mean0 = wave_sum(sm0) * (1.f / 512.f), mean1 = wave_sum(sm1) * (1.f / 512.f);
        float q0 = 0.f, q1 = 0.f;
#pragma unroll
        for (int k = 0; k < 4; ++k) {
          float t = lo2f(i0[k]) - mean0; q0 += t * t; t = hi2f(i0[k]) - mean0; q0 += t * t;
          t = lo2f(i1[k]) - mean1; q1 += t * t; t = hi2f(i1[k]) - mean1; q1 += t * t;
        }
        const float isd0 = rcp(rbf(__builtin_amdgcn_sqrtf(rbf(rbf(wave_sum(q0) * (1.f / 512.f)) + 1e-5f))));
        const float isd1 = rcp(rbf(__builtin_amdgcn_sqrtf(rbf(rbf(wave_sum(q1) * (1.f / 512.f)) + 1e-5f))));
        ln0 = rbf(rbf(lo2f(xd0) - rbf(mean0)) * isd0); ln1 = rbf(rbf(lo2f(xd1) - rbf(mean1)) * isd1);
      }
      asm volatile("" : "+v"(a0), "+v"(a1), "+v"(ln0), "+v"(ln1));
      v4u bj[8];
#pragma unroll
      for (int s = 0; s < 2; ++s)
#pragma unroll
        for (int d = 0; d < 4; ++d)
#pragma unroll
          for (int t = 0; t < 4; ++t)
            bj[s * 4 + t][d] = (t & 1) ? pk_hi(wr[s * 8 + 2 * d][t >> 1], wr[s * 8 + 2 * d + 1][t >> 1]) : pk_lo(wr[s * 8 + 2 * d][t >> 1], wr[s * 8 + 2 * d + 1][t >> 1]);
      if (isQ) {
        asm volatile("" : "+v"(bj[0]), "+v"(bj[1]), "+v"(bj[2]), "+v"(bj[3]), "+v"(bj[4]), "+v"(bj[5]), "+v"(bj[6]), "+v"(bj[7]));
        STAMP(1);
        asm volatile("" : "+v"(ep));
        const unsigned tag = ep + 1u;
        v4u t[2 * FH];
        for (unsigned spins = 0; (unsigned)__builtin_amdgcn_s_memrealtime() - tstart < (unsigned)HT && ++spins < 4096u;) __builtin_amdgcn_s_sleep(1);
#pragma unroll 1
        for (unsigned spins = 0;;) {
          bool ok = true;
          PSTAMP(11);
#pragma unroll
          for (int k = 0; k < 2 * FH; ++k) t[k] = LD128(rw, lane * 16 + k * 1024, W_HG + w * (2048 * FH), AUX_SC1);
#pragma unroll
          for (int k = 0; k < 2 * FH; ++k) ok &= (t[k][1] == tag) & (t[k][3] == tag);
          if (__all(ok) || ++spins > SPIN_G) break;
          asm volatile("" ::: "memory");
        }
        STAMP(2);
        float sa0 = 0.f, sa1 = 0.f, ca0 = 0.f, ca1 = 0.f;
#pragma unroll
        for (int k = 0; k < FH; ++k) { sa0 += __uint_as_float(t[k][0]); sa1 += __uint_as_float(t[k][2]); ca0 += __uint_as_float(t[FH + k][0]); ca1 += __uint_as_float(t[FH + k][2]); }
        const unsigned hh = cvtpk(rbf(ln0 * rbf(1.f + rbf(ca0))) + rbf(sa0), rbf(ln1 * rbf(1.f + rbf(ca1))) + rbf(sa1));
        *(LAS u16*)(L + L_HX + w * 256 + lane * 2) = (u16)hh; *(LAS u16*)(L + L_HX + w * 256 + 128 + lane * 2) = (u16)(hh >> 16);
        asm volatile("" ::: "memory");
        a0 = *(LAS v4u*)(L + L_HX + w * 256 + (n & 1) * 128 + g * 16); a1 = *(LAS v4u*)(L + L_HX + w * 256 + (n & 1) * 128 + 64 + g * 16);
      }
      f32x4 y[4];
#pragma unroll
      for (int t = 0; t < 4; ++t) y[t] = mfma(a1, bj[4 + t], mfma(a0, bj[t], f32x4{0.f, 0.f, 0.f, 0.f}));
      if (lane < 32) *(LAS f32x4*)(L + L_RED + w * 512 + lane * 16) = g ? f32x4{y[0][1], y[1][1], y[2][1], y[3][1]} : f32x4{y[0][0], y[1][0], y[2][0], y[3][0]};
      asm volatile("s_waitcnt lgkmcnt(0)" ::: "memory");
      unsigned old = 0;
      if (lane == 0) old = __hip_atomic_fetch_add((LAS unsigned*)(L + L_JCNT), 1u, __ATOMIC_RELAXED, __HIP_MEMORY_SCOPE_WORKGROUP);
      if (__builtin_amdgcn_readfirstlane(old) == (unsigned)(nact - 1)) {
        asm volatile("" : "+v"(ep) :: "memory");
        const unsigned tag = ep + 1u;
        f32x2 s = {0.f, 0.f};
#pragma unroll
        for (int ww = 0; ww < 8 / FH; ++ww) s += *(LAS f32x2*)(L + L_RED + ww * 512 + lane * 8);
        if (isH) {
          GAS u64* o = (GAS u64*)(p.ws + W_HG + (hl & 7) * (2048 * FH) + ((hl >> 3) * FH + hf) * 1024 + (lane & 31) * 32 + (lane >> 5) * 8);
          __hip_atomic_store(o, ((u64)tag << 32) | __float_as_uint(s[0]), RLX_AGENT);
          __hip_atomic_store(o + 2, ((u64)tag << 32) | __float_as_uint(s[1]), RLX_AGENT);
        } else {
#pragma unroll
          for (int ww = 8 / FH; ww < 8; ++ww) s += *(LAS f32x2*)(L + L_RED + ww * 512 + lane * 8);
          const int o = (isQ ? W_QG + (lane >> 5) * 16384 + (bid - Q0) * 256 : W_MG + (lane >> 5) * 8192 + (bid - M0) * 256) + (lane & 31) * 8;
          __hip_atomic_store((GAS u64*)(p.ws + o), ((u64)tag << 32) | cvtpk(s[0], s[1]), RLX_AGENT);
        }
        *(volatile LAS unsigned*)(L + L_JCNT) = 9u;
        STAMP(3);
      }
    }
    asm volatile("" : "+v"(ep));
    const unsigned tag = ep + 1u;
    if (lane == 0) __hip_atomic_fetch_add((LAS unsigned*)(L + L_ECNT), 1u, __ATOMIC_RELAXED, __HIP_MEMORY_SCOPE_WORKGROUP);
    if (w >= 8 - NQP) {
      for (unsigned spins = 0; (unsigned)__builtin_amdgcn_s_memrealtime() - tstart < (unsigned)(QT + (7 - w) * QSTAG) && ++spins < 4096u;) __builtin_amdgcn_s_sleep(1);
#pragma unroll 1
      for (unsigned spins = 0; lds_get(L, L_QRDY) == 0u;) {
        PSTAMP(10);
        const v4u t0 = LD128(rw, lane * 16, W_QG + b * 16384 + kvh * 2048, AUX_SC1), t1 = LD128(rw, lane * 16 + 1024, W_QG + b * 16384 + kvh * 2048, AUX_SC1);
        if (__all((t0[1] == tag) & (t0[3] == tag) & (t1[1] == tag) & (t1[3] == tag)) || ++spins > SPIN_G) {
          *(LAS v2u*)(L + L_Q + lane * 8) = v2u{t0[0], t0[2]}; *(LAS v2u*)(L + L_Q + 512 + lane * 8) = v2u{t1[0], t1[2]};
          asm volatile("s_waitcnt lgkmcnt(0)" ::: "memory");
          *(volatile LAS unsigned*)(L + L_QRDY) = 1u;
          STAMP(4);
        }
        asm volatile("" ::: "memory");
      }
    }
    if (w == 1) {
      if (CLRW == 0) {
      lds_wait<8>(L, L_QRDY, 1);
      const v4u s4 = {SENT, SENT, SENT, SENT};
      if (!(NOCLR & 1)) { ST128(s4, rw, lane * 16, W_PO + bid * 2048, AUX_SC1);
      ST128(s4, rw, lane * 16 + 1024, W_PO + bid * 2048, AUX_SC1); }
      const int oo = hasC && !(NOCLR & 2) ? W_OP + (b * 16 * 112 + fblk) * 128 : (int)OOB;
      ST128(s4, rw, r8 * (112 * 128) + c8 * 16, oo, AUX_SC1);
      ST128(s4, rw, (8 + r8) * (112 * 128) + c8 * 16, oo, AUX_SC1);
      if (!(NOCLR & 4)) ST128(s4, rw, lane < 4 ? W_ML + bid * 128 + lane * 16 : lane < 12 ? W_AB + ((b * 8 + kvh) * 512 + cblk * 32) * 4 + (lane - 4) * 16 : (int)OOB, 0, AUX_SC1);
      waitvm<0>();
      *(volatile LAS unsigned*)(L + L_CLRD) = 1u;
      } else { lds_wait<8>(L, L_FULL, 8 * CLT); lds_wait<2>(L, L_CLRD, 1); }
      lds_wait<2>(L, L_ECNT, 8);
      if (lane == 0 && !(NOCLR & 8)) __hip_atomic_store((GAS u64*)(p.ws + W_CL + bid * 8), ((u64)tag << 32) | (tag ^ 0x5bd1e995u), RLX_AGENT);
    }
    unsigned wo_r[16], w1_r[16]; v2u w2_r[4];
    {
      const rsrc_t ro = mkrs(p.wo, (ABL & 1) ? 0 : 4096 * 512 * 2), r1 = mkrs(p.w1, hasC && !(ABL & 2) ? 512 * 1792 * 2 : 0), r2 = mkrs(p.w2, hasC && !(ABL & 4) ? 1792 * 512 * 2 : 0);
#define WT(i) (8 * WBASE + (9 * w + (i)) * WSP)
#pragma unroll
      for (int i = 0; i < 16; ++i) { if ((i & 3) == 0) lds_wait<8>(L, L_FULL, WT(i >> 2)); wo_r[i] = LD32(ro, g * 1024 + n * 4, (kvh * 512 + w * 64 + 4 * i) * 1024 + cblk * 64, AUX_W); }
#pragma unroll
      for (int i = 0; i < 16; ++i) { if ((i & 3) == 0) lds_wait<8>(L, L_FULL, WT(4 + (i >> 2))); w1_r[i] = __builtin_amdgcn_raw_buffer_load_b16(r1, g * 3584 + n * 2, (w * 64 + 4 * i) * 3584 + fblk * 32, AUX_W); }
      lds_wait<8>(L, L_FULL, WT(8));
#pragma unroll
      for (int i = 0; i < 4; ++i) w2_r[i] = LD64(r2, (4 * g + i) * 1024 + n * 8, fblk * 16384 + w * 128, AUX_W);
    }
    const int col = w * 64 + 4 * n + g;
    const unsigned xin = __builtin_amdgcn_raw_buffer_load_b16(mkrs(p.x, 2048), col * 2, b * 1024, 0);
    v2u mg[4];
#pragma unroll 1
    for (unsigned spins = 0;;) {
      bool ok = true;
#pragma unroll
      for (int k = 0; k < 3; ++k) mg[k] = LD64(rw, (col >> 1) * 8 + k * 2048, W_MG + b * 8192, AUX_SC1);
      mg[3] = LD64(rw, (g >> 1) * 8, W_MG + b * 8192 + 6144 + ocol * 4, AUX_SC1);
      const v2u e = LD64(rw, (lane & 31) * 8, W_CL + w * 256, AUX_SC1);
#pragma unroll
      for (int k = 0; k < 4; ++k) ok &= mg[k][1] == tag;
      if (!(NOCLR & 8)) ok &= (e[0] == (tag ^ 0x5bd1e995u)) & (e[1] == tag);
      if (__all(ok) || ++spins > SPIN_G) break;
      __builtin_amdgcn_s_sleep(32); asm volatile("" ::: "memory");
    }
    v4u bo[4], b1[2]; v2u b2[4];
#pragma unroll
    for (int a = 0; a < 8; ++a) {
      bo[(a >> 2) * 2][a & 3] = pk_lo(wo_r[2 * a], wo_r[2 * a + 1]); bo[(a >> 2) * 2 + 1][a & 3] = pk_hi(wo_r[2 * a], wo_r[2 * a + 1]);
      b1[a >> 2][a & 3] = pk_lo(w1_r[2 * a], w1_r[2 * a + 1]);
    }
#pragma unroll
    for (int t = 0; t < 4; ++t)
#pragma unroll
      for (int d = 0; d < 2; ++d) b2[t][d] = (t & 1) ? pk_hi(w2_r[2 * d][t >> 1], w2_r[2 * d + 1][t >> 1]) : pk_lo(w2_r[2 * d][t >> 1], w2_r[2 * d + 1][t >> 1]);
    float xf = lo2f(xin), ga = (g & 1) ? hi2f(mg[0][0]) : lo2f(mg[0][0]), sb = (g & 1) ? hi2f(mg[1][0]) : lo2f(mg[1][0]);
    float cb1 = rbf(1.f + ((g & 1) ? hi2f(mg[2][0]) : lo2f(mg[2][0]))), gv = (g & 1) ? hi2f(mg[3][0]) : lo2f(mg[3][0]);
    asm volatile("" : "+v"(bo[0]), "+v"(bo[1]), "+v"(bo[2]), "+v"(bo[3]), "+v"(b1[0]), "+v"(b1[1]), "+v"(b2[0]), "+v"(b2[1]), "+v"(b2[2]), "+v"(b2[3]));
    asm volatile("" : "+v"(xf), "+v"(ga), "+v"(sb), "+v"(cb1), "+v"(gv));

    int passv = (DRYRUN && w == 0) ? 0 : 1;
    for (;;) {
      asm volatile("" : "+v"(passv));
      const int pass = __builtin_amdgcn_readfirstlane(passv);
      const rsrc_t rt = mkrs(p.ws, pass ? W_END : 0);
      if (pass) { STAMP(5); bar(); STAMP(12); }
      {
        v4u o4[4];
        const int po = W_PO + (bid & ~15) * 2048 + w * 256, mo = W_ML + (bid & ~15) * 128 + (w >> 1) * 16;
        v2u ml = LD64(rt, (n ^ g) * 128, mo, AUX_SC1);
        if (MLONLY != 2) {
#pragma unroll
          for (int i = 0; i < 4; ++i) o4[i] = LD128(rt, (4 * i + g) * 2048 + n * 16, po, AUX_SC1);
        }
        unsigned spins = 0;
#pragma unroll 1
        for (;;) {
          const bool badm = max(ml[0], ml[1]) == SENT;
          if (!__any(badm) || ++spins > SPIN_G) break;
          if (POLL_SLEEP) __builtin_amdgcn_s_sleep(POLL_SLEEP);
          asm volatile("" ::: "memory");
          if (badm) ml = LD64(rt, (n ^ g) * 128, mo, AUX_SC1);
          if (!MLONLY) {
#pragma unroll
            for (int i = 0; i < 4; ++i) if (umax4(o4[i]) == SENT) o4[i] = LD128(rt, (4 * i + g) * 2048 + n * 16, po, AUX_SC1);
          }
        }
        if (MLONLY) {
#pragma unroll
          for (int i = 0; i < 4; ++i) if (MLONLY == 2 || umax4(o4[i]) == SENT) o4[i] = LD128(rt, (4 * i + g) * 2048 + n * 16, po, AUX_SC1);
        }
        const float mi = __uint_as_float(ml[0]);
        float M = fmaxf(mi, dpp<0x128>(mi)); M = fmaxf(M, dpp<0x124>(M)); M = fmaxf(M, dpp<0x122>(M)); M = fmaxf(M, dpp<0x121>(M));
        const float e = __builtin_amdgcn_exp2f((mi - M) * L2E);
        const float wg = e * rcp(row_sum(e * __uint_as_float(ml[1])));
        float w0 = dpp<0x150>(wg), w1 = dpp<0x154>(wg), w2 = dpp<0x158>(wg), w3 = dpp<0x15C>(wg);
        asm volatile("" : "+v"(w0), "+v"(w1), "+v"(w2), "+v"(w3));
#pragma unroll 1
        for (;;) {
          bool bad[4];
#pragma unroll
          for (int i = 0; i < 4; ++i) bad[i] = umax4(o4[i]) == SENT;
          if (!__any(bad[0] | bad[1] | bad[2] | bad[3]) || ++spins > SPIN_G) break;
          asm volatile("" ::: "memory");
#pragma unroll
          for (int i = 0; i < 4; ++i) if (bad[i]) o4[i] = LD128(rt, (4 * i + g) * 2048 + n * 16, po, AUX_SC1);
        }
        if (pass) STAMP(6);
        const f32x4 o = (w0 * __builtin_bit_cast(f32x4, o4[0]) + w1 * __builtin_bit_cast(f32x4, o4[1])) + (w2 * __builtin_bit_cast(f32x4, o4[2]) + w3 * __builtin_bit_cast(f32x4, o4[3]));
        v4u a0, a1; afrag(tr4(o), a0, a1);
        const f32x4 z = {0.f, 0.f, 0.f, 0.f};
        const f32x4 y0 = mfma(a1, bo[2], mfma(a0, bo[0], z)), y1 = mfma(a1, bo[3], mfma(a0, bo[1], z));
        if (lane < 16) *(LAS f32x2*)(L + L_RAB + w * 128 + n * 8) = f32x2{y0[0], y1[0]};
        if (pass) { STAMP(13); bar(); }
        if (w == 0) {
          float t = tr4(*(LAS f32x4*)(L + L_RAB + r8 * 128 + c8 * 16));
          t += dpp<0x128>(t);
          __builtin_amdgcn_raw_buffer_store_b32(nosent(__float_as_uint(t)), rt, n < 8 ? n * 16 + g * 4 : (int)OOB, W_AB + ((b * 8 + kvh) * 512 + cblk * 32) * 4, AUX_SC1);
          if (pass) STAMP(7);
        }
      }
      {
        if (CPRE) __builtin_amdgcn_s_sleep(CPRE);
        v4u a[2];
#pragma unroll
        for (int i = 0; i < 2; ++i) a[i] = LD128(rt, (4 * i + g) * 2048 + n * 16, W_AB + b * 16384 + w * 256, AUX_SC1);
#pragma unroll 1
        for (unsigned spins = 0;;) {
          const bool bad0 = umax4(a[0]) == SENT, bad1 = umax4(a[1]) == SENT;
          if (!__any(bad0 | bad1) || ++spins > SPIN_G) break;
          if (POLL_SLEEP2) __builtin_amdgcn_s_sleep(POLL_SLEEP2);
          asm volatile("" ::: "memory");
          if (bad0) a[0] = LD128(rt, g * 2048 + n * 16, W_AB + b * 16384 + w * 256, AUX_SC1);
          if (bad1) a[1] = LD128(rt, (4 + g) * 2048 + n * 16, W_AB + b * 16384 + w * 256, AUX_SC1);
        }
        if (pass) STAMP(8);
        const float x1 = rbf(xf + rbf(ga * rbf(tr4(__builtin_bit_cast(f32x4, a[0]) + __builtin_bit_cast(f32x4, a[1])))));
        const float pv = __builtin_bit_cast(float, __builtin_amdgcn_readfirstlane(__builtin_bit_cast(int, x1)));
        const float dx = x1 - pv;
        float ss = tr32(dx, dx * dx); ss = row_sum(tr16(ss, ss));
        if ((lane & 31) == 0) *(LAS f32x2*)(L + L_ST + w * 16 + (lane >> 5) * 8) = f32x2{ss, pv};
        if (w == xl && n == j16) *(LAS float*)(L + L_X1 + g * 4) = x1;
        if (pass) { STAMP(14); bar(); }
        const f32x4 st = *(LAS f32x4*)(L + L_ST + c8 * 16);
        const float mw = st[1] + st[0] * (1.f / 64.f), c0 = dpp<0x150>(mw), dm = mw - c0;
        float t1 = dm, t2 = (st[2] - st[0] * st[0] * (1.f / 64.f)) + 64.f * dm * dm;
        t1 += dpp<0xB1>(t1); t2 += dpp<0xB1>(t2); t1 += dpp<0x4E>(t1); t2 += dpp<0x4E>(t2); t1 += dpp<0x141>(t1); t2 += dpp<0x141>(t2);
        t1 *= 0.125f;
        const float mean = rbf(c0 + t1), var = rbf(fmaxf(t2 * (1.f / 512.f) - t1 * t1, 0.f));
        const float isd = rcp(rbf(__builtin_amdgcn_sqrtf(rbf(var + 1e-5f))));
        v4u a0, a1; afrag(rbf(rbf(rbf(x1 - mean) * isd) * cb1) + sb, a0, a1);
        const f32x4 y = mfma(a1, b1[1], mfma(a0, b1[0], f32x4{0.f, 0.f, 0.f, 0.f}));
        if (lane < 16) *(LAS float*)(L + L_RC + n * 32 + w * 4) = y[0];
        if (pass) { STAMP(15); bar(); }
        const f32x2 s = *(LAS f32x2*)(L + L_RC + lane * 8);
        float t0 = s[0] + s[1];
        t0 += dpp<0xB1>(t0); t0 += dpp<0x4E>(t0);
        const float hv = silu_f(rbf(t0));
        const v2u ah = {cvtpk(hv, dpp<0x104>(hv)), cvtpk(dpp<0x108>(hv), dpp<0x10C>(hv))};
        v4u o;
#pragma unroll
        for (int t = 0; t < 4; ++t) o[t] = nosent(__float_as_uint(mfma16(ah, b2[t])[0]));
        ST128(o, rt, lane < 16 && hasC ? (n >> 3) * (112 * 128) + (n & 7) * 16 : (int)OOB, W_OP + ((b * 16 + 2 * w) * 112 + fblk) * 128, AUX_SC1);
        if (pass) TSTAMP(9);
      }
      if (w == 0) {
        const int oo = W_OP + (b * 16 + (ocol >> 5)) * (112 * 128) + (ocol & 31) * 4;
        const int o0 = lane < 56 ? lane * 128 : (int)OOB, o1 = lane < 56 ? (lane + 56) * 128 : (int)OOB;
        const float xv = *(LAS float*)(L + L_X1 + g * 4);
        if (DPRE) __builtin_amdgcn_s_sleep(DPRE);
        v4u a0 = LD128(rt, o0, oo, AUX_SC1), a1 = LD128(rt, o1, oo, AUX_SC1);
#pragma unroll 1
        for (unsigned spins = 0;;) {
          const bool bad0 = umax4(a0) == SENT, bad1 = umax4(a1) == SENT;
          if (!__any(bad0 | bad1) || ++spins > SPIN_G) break;
          asm volatile("" ::: "memory");
          if (bad0) a0 = LD128(rt, o0, oo, AUX_SC1);
          if (bad1) a1 = LD128(rt, o1, oo, AUX_SC1);
        }
        if (pass) TSTAMP(10);
        const float y2 = row_sum(tr4(__builtin_bit_cast(f32x4, a0) + __builtin_bit_cast(f32x4, a1)));
        const unsigned r = cvtpk(xv + rbf(gv * rbf(y2)), 0.f);
        if (pass && n == 0) *(GAS u16*)(p.out + b * 512 + ocol + g) = (u16)r;
        __builtin_amdgcn_raw_buffer_store_b32(tag, rt, (lane == 0 && bid == 0) ? 0 : (int)OOB, W_EPOCH, AUX_SC1);
        if (pass) TSTAMP(11);
      }
      if (pass) break;
      passv = 1;
    }
    STAMP_OUT();
    return;
  }

  {
    const rsrc_t rk = mkrs(p.K + (size_t)bid * (NUNIT * 4096), NUNIT * 8192), rv = mkrs(p.V + (size_t)bid * (NUNIT * 4096), NUNIT * 8192);
    const int k0 = g * 256 + ((n ^ g) * 16), k1 = k0 ^ 64, k2 = k0 ^ 128, k3 = k0 ^ 192, ve = g * 256 + ((n ^ (2 * g)) * 16), vo = ve ^ 128;
    unsigned freed = 0; int slot = 0, pub = 0;
    const int deep = (QHOLD && isQ) ? QCATCH : (bid >= M0 && bid < JEND) ? MCATCH : EARLY;
    const unsigned tl0 = (unsigned)__builtin_amdgcn_s_memrealtime();
    STAMP(3);
#define PACE(done) do { if (mode == 0) { waitvm<DSLOW - 4>(); pub = max(pub, (done) - (DSLOW - 4)); } else if (mode == 2) { waitvm<DDEEP - 4>(); pub = max(pub, (done) - (DDEEP - 4)); } \
                        else { waitvm<DEPTH - 4>(); pub = max(pub, (done) - (DEPTH - 4)); } *FULL = pub; } while (0)
#pragma unroll 1
    for (int u = 0; u < NUNIT; ++u) {
      if (QHOLD && u == QHOLD - 1 && isQ) { waitvm<0>(); pub = 16 * u; *FULL = pub; lds_wait<1>(L, L_JCNT, 9); }
      if (LPRIO && u == 1) __builtin_amdgcn_s_setprio(LPRIO);
      if (u >= NSLOT && freed < (unsigned)(u - NSLOT + 1)) {
#pragma unroll 1
        for (unsigned spins = 0;;) {
          const v4u f = *(volatile LAS v4u*)(L + L_FREE);
          freed = __builtin_amdgcn_readfirstlane(min(min(f[0], f[1]), min(f[2], f[3])));
          if (freed >= (unsigned)(u - NSLOT + 1)) break;
          if (spins == 0) { if (u <= NSLOT + NPRE) STAMP(4); STAMP(2); }
          if (++spins > SPIN_L) { freed = SENT; break; }
        }
      }
      int mode = u < deep ? 2 : 1;
      if (TAU && u >= 10) { const int err = (int)((unsigned)__builtin_amdgcn_s_memrealtime() - tl0) - (TA0 + (u * TAU) / 16); mode = err > EHI ? 2 : err < -ELO ? 0 : 1; }
      const unsigned lb = slot * 16384;
      dma4(rk, k0, k1, k2, k3, u * 8192, lb); PACE(16 * u + 4);
      dma4(rk, k0, k1, k2, k3, u * 8192 + 4096, lb + 4096); PACE(16 * u + 8);
      dma4(rv, ve, vo, ve, vo, u * 8192, lb + 8192); PACE(16 * u + 12);
      dma4(rv, ve, vo, ve, vo, u * 8192 + 4096, lb + 12288); PACE(16 * u + 16);
      slot = (slot + 1 == NSLOT) ? 0 : slot + 1;
    }
    if (DDEEP > 28) { waitvm<24>(); pub = max(pub, 16 * NUNIT - 24); *FULL = pub; }
    waitvm<16>(); pub = max(pub, 16 * NUNIT - 16); *FULL = pub;
    waitvm<8>(); *FULL = 16 * NUNIT - 8;
    waitvm<0>(); *FULL = 16 * NUNIT;
    STAMP(1);
    STAMP_OUT();
  }
}

extern "C" void kernel_launch(void* const* d_in, const int* in_sizes, int n_in, void* d_out, int out_size, void* d_ws, size_t ws_size, hipStream_t stream) {
  Params p;
  p.x = (const u16*)d_in[0]; p.K = (const u16*)d_in[1]; p.V = (const u16*)d_in[2]; p.wq = (const u16*)d_in[3]; p.wo = (const u16*)d_in[4];
  p.w1 = (const u16*)d_in[6]; p.w2 = (const u16*)d_in[7]; p.c = (const u16*)d_in[8]; p.wmod = (const u16*)d_in[9];
  p.out = (u16*)d_out; p.ws = (unsigned char*)d_ws;
  (void)hipFuncSetAttribute((const void*)adaln_block_kernel, hipFuncAttributeMaxDynamicSharedMemorySize, L_END);
  hipLaunchKernelGGL(adaln_block_kernel, dim3(NWG), dim3(NTHR), L_END, stream, p);
}
```

```cpp
#include <hip/hip_runtime.h>
#include <hip/hip_bf16.h>
#include <stdint.h>

typedef unsigned short u16;
typedef unsigned long long u64;
typedef unsigned v4u __attribute__((ext_vector_type(4)));
typedef unsigned v2u __attribute__((ext_vector_type(2)));
typedef float f32x4 __attribute__((ext_vector_type(4)));
typedef float f32x2 __attribute__((ext_vector_type(2)));
typedef short bf16x8 __attribute__((ext_vector_type(8)));
typedef short s16x4 __attribute__((ext_vector_type(4)));
typedef __bf16 bf16x2_t __attribute__((ext_vector_type(2)));
typedef __amdgpu_buffer_rsrc_t rsrc_t;
#define GAS __attribute__((address_space(1)))
#define LAS __attribute__((address_space(3)))
#define RLX_AGENT __ATOMIC_RELAXED, __HIP_MEMORY_SCOPE_AGENT

constexpr int NWG = 256, NWAVE = 14, NTHR = NWAVE * 64;
constexpr int NUNIT = 64;
constexpr int NSLOT = 16;
constexpr int NPK = 3;
constexpr int FH = 2;
constexpr int Q0 = 16 * FH, M0 = Q0 + 64, JEND = M0 + 32;
constexpr int DEPTH = 21, DDEEP = 32;
constexpr int QCATCH = 36, NORM = 6;
constexpr int CLKM = 23, HT = 380, QT = 660;
constexpr int WBASE = 240, WSP = 3;
constexpr int PU0 = 11, PUS = 8, PLAG = 4, PEND = 4, PNS = 16, PMAR = 22, PREC = 12, PH0 = 3, PH1 = 8;
constexpr int HL = 5, HSL = 4, DA = 140, DC = 240, DPRE = 9;
constexpr unsigned SPIN_G = 1u << 19, SPIN_L = 1u << 26;

constexpr unsigned SENT = 0xFFFFFFFFu, OOB = 0x7FFFFFF0u;
constexpr int AUX_NT = 2, AUX_SC1 = 16;
constexpr int W_EPOCH = 0, W_CL = 0x800  , W_QG = 0x2000  , W_MG = 0xA000  ,
              W_AB = 0x10000  , W_ML = 0x18000  , W_PO = 0x20000  ,
              W_OP = 0xA0000  , W_HG = 0x110000  ,
              W_HU = 0x118000  , W_PR = 0x120000  , W_END = 0x120400;
constexpr int L_SC = NSLOT * 8192  , L_CTRL = L_SC + 16384,
              L_FULL = L_CTRL, L_QRDY = L_CTRL + 4, L_JCNT = L_CTRL + 8, L_ECNT = L_CTRL + 12, L_FREE = L_CTRL + 16, L_CLRD = L_CTRL + 32,
              L_VCNT = L_CTRL + 36, L_ADV = L_CTRL + 40, L_CCNT = L_CTRL + 44, L_Q = L_CTRL + 64, L_RED = L_Q + 1024, L_HX = L_RED + 4096,
              L_X1 = L_HX  , L_RAB = L_HX + 2048, L_ST = L_RAB + 2048, L_RC = L_ST + 128, L_NUL = L_RC + 1024,
              L_MX = L_NUL + 64, L_TS = L_MX + 128, L_END = L_TS + 512;
static_assert(L_END <= 160 * 1024 && L_END > 80 * 1024, "one workgroup per CU");

__device__ __forceinline__ float lo2f(unsigned u) { return __builtin_bit_cast(float, u << 16); }
__device__ __forceinline__ float hi2f(unsigned u) { return __builtin_bit_cast(float, u & 0xffff0000u); }
__device__ __forceinline__ unsigned cvtpk(float lo, float hi) { f32x2 v = {lo, hi}; return __builtin_bit_cast(unsigned, __builtin_convertvector(v, bf16x2_t)); }
__device__ __forceinline__ float rbf(float x) { return lo2f(cvtpk(x, x)); }
__device__ __forceinline__ float rcp(float x) { return __builtin_amdgcn_rcpf(x); }
__device__ __forceinline__ float silu_f(float x) { return x * rcp(1.f + __builtin_amdgcn_exp2f(x * -1.4426950408889634f)); }
__device__ __forceinline__ float silu_x(float x) {
  const float d = 1.f + __builtin_amdgcn_exp2f(x * -1.4426950408889634f), r = rcp(d), q = x * r;
  return __builtin_amdgcn_div_fixupf(__builtin_fmaf(__builtin_fmaf(-q, d, x), r, q), d, x);
}
__device__ __forceinline__ f32x4 mfma(v4u a, v4u b, f32x4 c) {
  return __builtin_amdgcn_mfma_f32_16x16x32_bf16(__builtin_bit_cast(bf16x8, a), __builtin_bit_cast(bf16x8, b), c, 0, 0, 0);
}
__device__ __forceinline__ unsigned pk_lo(unsigned r0, unsigned r1) { return __builtin_amdgcn_perm(r1, r0, 0x05040100u); }
__device__ __forceinline__ unsigned pk_hi(unsigned r0, unsigned r1) { return __builtin_amdgcn_perm(r1, r0, 0x07060302u); }
__device__ __forceinline__ unsigned nosent(unsigned d) { return d < 0xFFFFFFFEu ? d : 0xFFFFFFFEu; }
template <int CTRL> __device__ __forceinline__ float dpp(float x) {
  return __builtin_bit_cast(float, __builtin_amdgcn_mov_dpp(__builtin_bit_cast(int, x), CTRL, 0xf, 0xf, true));
}
__device__ __forceinline__ float row_sum(float x) { x += dpp<0x128>(x); x += dpp<0x124>(x); x += dpp<0x122>(x); x += dpp<0x121>(x); return x; }
__device__ __forceinline__ float tr32(float a, float b) {
  auto s = __builtin_amdgcn_permlane32_swap(__float_as_uint(a), __float_as_uint(b), false, false);
  return __uint_as_float(s[0]) + __uint_as_float(s[1]);
}
__device__ __forceinline__ float tr16(float a, float b) {
  auto s = __builtin_amdgcn_permlane16_swap(__float_as_uint(a), __float_as_uint(b), false, false);
  return __uint_as_float(s[0]) + __uint_as_float(s[1]);
}
__device__ __forceinline__ float xg_sum(float x) { x = tr16(x, x); return tr32(x, x); }
__device__ __forceinline__ float xg_max(float x) {
  auto s = __builtin_amdgcn_permlane16_swap(__float_as_uint(x), __float_as_uint(x), false, false);
  x = fmaxf(__uint_as_float(s[0]), __uint_as_float(s[1]));
  auto t = __builtin_amdgcn_permlane32_swap(__float_as_uint(x), __float_as_uint(x), false, false);
  return fmaxf(__uint_as_float(t[0]), __uint_as_float(t[1]));
}
__device__ __forceinline__ float wave_sum(float x) { return xg_sum(row_sum(x)); }
__device__ __forceinline__ unsigned umax4(v4u v) { return max(max(v[0], v[1]), max(v[2], v[3])); }
template <int N> __device__ __forceinline__ void waitvm() { asm volatile("s_waitcnt vmcnt(%0)" :: "n"(N) : "memory"); }
__device__ __forceinline__ void bar() { asm volatile("s_waitcnt lgkmcnt(0)\n\ts_barrier" ::: "memory"); }
__device__ __forceinline__ rsrc_t mkrs(const void* p, unsigned bytes) { return __builtin_amdgcn_make_buffer_rsrc((void*)p, 0, bytes, 0x00020000); }
__device__ __forceinline__ void dma4(rsrc_t rs, int v0, int v1, int v2, int v3, int soff, unsigned lds) {
  asm volatile("s_mov_b32 m0, %6\n\ts_nop 0\n\tbuffer_load_dwordx4 %0, %4, %5 offen nt lds\n\tbuffer_load_dwordx4 %1, %4, %5 offen offset:1024 nt lds\n\t"
               "buffer_load_dwordx4 %2, %4, %5 offen offset:2048 nt lds\n\tbuffer_load_dwordx4 %3, %4, %5 offen offset:3072 nt lds"
               :: "v"(v0), "v"(v1), "v"(v2), "v"(v3), "s"(rs), "s"(soff), "s"(lds) : "memory");
}
#define LD128(rs, vo, so, aux) __builtin_amdgcn_raw_buffer_load_b128(rs, vo, so, aux)
#define LD64(rs, vo, so, aux) __builtin_amdgcn_raw_buffer_load_b64(rs, vo, so, aux)
#define LD32(rs, vo, so, aux) __builtin_amdgcn_raw_buffer_load_b32(rs, vo, so, aux)
#define ST128(v, rs, vo, so, aux) __builtin_amdgcn_raw_buffer_store_b128(v, rs, vo, so, aux)
#define ST64(v, rs, vo, so, aux) __builtin_amdgcn_raw_buffer_store_b64(v, rs, vo, so, aux)

template <int CTRL> __device__ __forceinline__ unsigned dppu(unsigned x) { return (unsigned)__builtin_amdgcn_mov_dpp((int)x, CTRL, 0xf, 0xf, true); }
__device__ __forceinline__ void afrag(float x, v4u& a0, v4u& a1) {
  const unsigned P = cvtpk(x, dpp<0x101>(x));
  a0 = v4u{P, dppu<0x102>(P), dppu<0x104>(P), dppu<0x106>(P)};
  a1 = v4u{dppu<0x108>(P), dppu<0x10A>(P), dppu<0x10C>(P), dppu<0x10E>(P)};
}
__device__ __forceinline__ float tr4(f32x4 v) { return tr16(tr32(v[0], v[2]), tr32(v[1], v[3])); }
#define LP(off) ((LAS unsigned char*)(size_t)(unsigned)(off))
template <int V> struct IC { static constexpr int value = V; };
__device__ __forceinline__ int unit_addr(int u, int grp, int mem) { return (((u + grp * 5) & 63) << 4) | ((mem + u) & 15); }
struct Params { unsigned char* ws; const u16 *K, *V, *wq, *wmod, *x, *c; u16* out; const u16 *wo, *w1, *w2; };

__device__ __forceinline__ unsigned lds_get(LAS unsigned char* L, int off) { return __builtin_amdgcn_readfirstlane(*(volatile LAS unsigned*)(L + off)); }
template <int SL> __device__ __forceinline__ void lds_wait(LAS unsigned char* L, int off, unsigned want) {
#pragma unroll 1
  for (unsigned spins = 0; lds_get(L, off) < want && ++spins < SPIN_L;) if (SL) __builtin_amdgcn_s_sleep(SL);
  asm volatile("" ::: "memory");
}

__global__ void __launch_bounds__(NTHR, 4) __attribute__((aligned(4096))) adaln_block_kernel(Params) {
  extern __shared__ __attribute__((aligned(16))) unsigned char smem[];
  LAS unsigned char* L = (LAS unsigned char*)smem;
  const int tid = threadIdx.x, lane = tid & 63, w = __builtin_amdgcn_readfirstlane(tid >> 6);
  const int bid = blockIdx.x;
  typedef const __attribute__((address_space(4))) Params* kp_t;
  kp_t kp = (kp_t)__builtin_amdgcn_kernarg_segment_ptr();
  unsigned karg_touch;
  asm volatile("s_cmp_eq_u32 %2, 12\n\ts_cbranch_scc1 .Ladaln_init\n\ts_load_dword %0, %1, 0x0\n\ts_branch .Ladaln_sync\n.Ladaln_init:\n\t"
               "ds_write_b32 %3, %4\n\ts_waitcnt lgkmcnt(0)\n.Ladaln_sync:\n\ts_barrier"
               : "=&s"(karg_touch), "+s"(kp) : "s"(w), "v"(L_CTRL + (lane & 15) * 4), "v"(0) : "scc", "memory");
#define p (*kp)
#define ARGS_READY() asm volatile("s_waitcnt lgkmcnt(0)" :: "s"(karg_touch))
  const int xl = bid & 7, jl = bid >> 3, b = bid >> 7, kvh = (bid >> 4) & 7, mem = bid & 15, grp = bid >> 4;
  const int g = lane >> 4, n = lane & 15, r8 = lane >> 3, c8 = lane & 7;
  volatile LAS unsigned* FULL = (volatile LAS unsigned*)(L + L_FULL);
  constexpr float L2E = 1.4426950408889634f;
  const bool isH = bid < Q0, isQ = !isH && bid < M0;
  const bool isA = mem < 8;
  const int akv = xl, agrp = b * 8 + xl, aln = kvh;
  const bool hasC = !isA && kvh < 7;
  const int fblk = xl * 7 + kvh;
  const int ocol = xl * 64 + kvh * 8;

  if (w == 12) {
    ARGS_READY();
    const rsrc_t rw = mkrs(p.ws, W_END);
    const unsigned tag = LD32(rw, 0, W_EPOCH, AUX_SC1) + 1u;
    asm volatile("" :: "v"(tag) : "memory");
    if (lane == 0) __hip_atomic_fetch_add((LAS unsigned*)(L + L_ECNT), 1u, __ATOMIC_RELAXED, __HIP_MEMORY_SCOPE_WORKGROUP);
    const unsigned tb = (unsigned)__builtin_amdgcn_s_memrealtime();
    float Ta = 0.f, tau = 0.f; int Ua = -1;
#pragma unroll 1
    for (int u = PU0; u < NUNIT - PEND; ++u) {
      lds_wait<2>(L, L_FULL, 16 * u);
      const unsigned t = (unsigned)__builtin_amdgcn_s_memrealtime();
      const float lag = (float)(int)(t - tb) - (Ta + (float)(u - Ua) * tau);
      if (tau > 0.f && lag > PMAR) *(volatile LAS unsigned*)(L + L_ADV) = (unsigned)(2 * u + PH0 + min((int)(lag * (2.f / PREC)), PH1));
      const int r = u - PU0, k = r / PUS, ph = r % PUS;
      if (ph == 0) ST64((v2u{t, tag}), rw, ((jl & 27) == 16 && lane == 0) ? (((jl >> 2) & 1) * 8 + xl) * 8 : (int)OOB, W_PR + k * 128, AUX_SC1);
      if (ph == PLAG && u < NUNIT - 4) {
        const v2u e = LD64(rw, n * 8, W_PR + k * 128, AUX_SC1);
        const bool ok = e[1] == tag;
        const float cnt = row_sum(ok ? 1.f : 0.f), T = row_sum(ok ? (float)(int)(e[0] - tb) : 0.f) * rcp(cnt);
        if (__builtin_amdgcn_readfirstlane((int)cnt) >= PNS / 2) { if (Ua >= 0) tau = (T - Ta) * rcp((float)(u - PLAG - Ua)); Ta = T; Ua = u - PLAG; }
        u = max(u, (int)(lds_get(L, L_FULL) >> 4));
      }
    }
    return;
  }
  if (w >= 8 && w < 12) {
    const int c = w - 8;
    ARGS_READY();
    const int ka = n * 256 + (((4 * g) ^ (2 * (n & 7) + (n >> 3))) * 16);
    const int sw = L_SC + (g * 4 + (n & 3)) * 16 + (n >> 2) * 4, sr = L_SC + (g * 4 + (n & 3)) * 16;
    unsigned full = 0;
    v4u qf[4];
    float mx = -INFINITY;
    auto wait_full = [&](unsigned want) {
      for (unsigned spins = 0; full < want;) { full = __builtin_amdgcn_readfirstlane(*FULL); if (++spins > SPIN_L) full = SENT; }
      asm volatile("" ::: "memory");
    };
    auto release = [&](unsigned v) { asm volatile("s_waitcnt lgkmcnt(0)" ::: "memory"); *(volatile LAS unsigned*)(L + L_FREE + c * 4) = v; };
    auto meet = [&](unsigned want) {
      asm volatile("s_waitcnt lgkmcnt(0)" ::: "memory");
      if (lane == 0) __hip_atomic_fetch_add((LAS unsigned*)(L + L_CCNT), 1u, __ATOMIC_RELAXED, __HIP_MEMORY_SCOPE_WORKGROUP);
      lds_wait<1>(L, L_CCNT, want);
    };
    auto read_k = [&](int h, v4u (&kf)[8]) {
      LAS unsigned char* S = L + (h & (NSLOT - 1)) * 8192;
      wait_full(8 * h + 8);
#pragma unroll
      for (int i = 0; i < 8; ++i) kf[i] = *(LAS v4u*)(S + (ka ^ ((i & 3) * 16)) + (i >> 2) * 4096);
      release(min(h + 4, NUNIT));
    };
    auto scores = [&](int h, const v4u (&kf)[8]) {
      constexpr float SC = 0.08838834764831845f;
      f32x4 s0 = {0.f, 0.f, 0.f, 0.f}, s1 = {0.f, 0.f, 0.f, 0.f};
#pragma unroll
      for (int s = 0; s < 4; ++s) { s0 = mfma(kf[s], qf[s], s0); s1 = mfma(kf[4 + s], qf[s], s1); }
      const unsigned u0 = cvtpk(s0[0], s0[1]), u1 = cvtpk(s0[2], s0[3]), u2 = cvtpk(s1[0], s1[1]), u3 = cvtpk(s1[2], s1[3]);
      const unsigned us = (n & 8) ? ((n & 4) ? u3 : u2) : ((n & 4) ? u1 : u0);
      const unsigned ts = cvtpk(lo2f(us) * SC, hi2f(us) * SC);
      mx = fmaxf(mx, fmaxf(lo2f(ts), hi2f(ts)));
      *(LAS unsigned*)(L + sw + h * 256) = ts;
    };
    const rsrc_t rt = mkrs(p.ws, W_END);
    {
      v4u kp[NPK][8];
#pragma unroll
      for (int j = 0; j < NPK; ++j) read_k(4 * j + c, kp[j]);
      lds_wait<1>(L, L_QRDY, 1);
#pragma unroll
      for (int s = 0; s < 4; ++s) qf[s] = *(LAS v4u*)(L + L_Q + (n & 3) * 256 + g * 64 + s * 16);
#pragma unroll
      for (int j = 0; j < NPK; ++j) scores(4 * j + c, kp[j]);
    }
#pragma unroll 1
    for (int h = 4 * NPK + c; h < NUNIT; h += 4) { v4u kf[8]; read_k(h, kf); scores(h, kf); }
    mx = fmaxf(mx, dpp<0x124>(mx)); mx = fmaxf(mx, dpp<0x128>(mx)); mx = xg_max(mx);
    if (lane < 4) *(LAS float*)(L + L_MX + lane * 16 + c * 4) = mx;
    meet(4);
    const f32x4 m4 = *(LAS f32x4*)(L + L_MX + (n & 3) * 16);
    const float m = fmaxf(fmaxf(m4[0], m4[1]), fmaxf(m4[2], m4[3])), mb = m == -INFINITY ? 0.f : m;
    float l = 0.f;
#pragma unroll 4
    for (int j = 0; j < 16; ++j) {
      LAS unsigned* a = (LAS unsigned*)(L + sw + (4 * j + c) * 256);
      const unsigned ts = *a;
      const float p0 = __builtin_amdgcn_exp2f((lo2f(ts) - mb) * L2E), p1 = __builtin_amdgcn_exp2f((hi2f(ts) - mb) * L2E);
      l += p0 + p1;
      *a = cvtpk(p0, p1);
    }
    l += dpp<0x124>(l); l += dpp<0x128>(l); l = xg_sum(l);
    if (lane < 4) *(LAS float*)(L + L_MX + 64 + lane * 16 + c * 4) = l;
    meet(8);
    lds_wait<1>(L, L_CLRD, 1);
    if (c == 0) {
      const f32x4 l4 = *(LAS f32x4*)(L + L_MX + 64 + (n & 3) * 16);
      ST64((v2u{nosent(__float_as_uint(m)), nosent(__float_as_uint((l4[0] + l4[1]) + (l4[2] + l4[3])))}), rt, lane < 4 ? lane * 16 : (int)OOB, W_ML + bid * 128, AUX_SC1);
    }
    f32x4 acc0 = {0.f, 0.f, 0.f, 0.f}, acc1 = {0.f, 0.f, 0.f, 0.f};
    int ln = lane; asm volatile("" : "+v"(ln));
    const int g2 = ln >> 4, n2 = ln & 15, r7 = 4 * (g2 & 1) + (n2 >> 2);
    const int va0 = (4 * g2 + (n2 >> 2)) * 256 + (((4 * c + ((n2 >> 1) & 1)) ^ (2 * r7 + (g2 >> 1))) * 16) + 8 * (n2 & 1), va1 = va0 ^ 32;
    int po = n2 < 8 ? W_PO + bid * 2048 + (n2 & 3) * 512 + c * 128 + (n2 >> 2) * 64 + g2 * 16 : (int)OOB;
    asm volatile("" : "+v"(po));
#pragma unroll 1
    for (int u = 0; u < NUNIT; ++u) {
      LAS unsigned char* S = L + (u & (NSLOT - 1)) * 8192;
      v4u pf = *(LAS v4u*)(L + sr + u * 256);
      if (u == NUNIT - HL && c == 0) { __builtin_amdgcn_raw_buffer_store_b32(nosent((unsigned)__builtin_amdgcn_s_memrealtime()), rt, lane == 0 ? 0 : (int)OOB, W_HU + bid * 128, AUX_SC1); }
      asm volatile("" : "+v"(pf));
      wait_full(8 * (NUNIT + u) + 8);
      const v2u a00 = __builtin_bit_cast(v2u, __builtin_amdgcn_ds_read_tr16_b64_v4i16((LAS s16x4*)(S + va0)));
      const v2u a01 = __builtin_bit_cast(v2u, __builtin_amdgcn_ds_read_tr16_b64_v4i16((LAS s16x4*)(S + va0 + 4096)));
      const v2u a10 = __builtin_bit_cast(v2u, __builtin_amdgcn_ds_read_tr16_b64_v4i16((LAS s16x4*)(S + va1)));
      const v2u a11 = __builtin_bit_cast(v2u, __builtin_amdgcn_ds_read_tr16_b64_v4i16((LAS s16x4*)(S + va1 + 4096)));
      release(NUNIT + u + 1);
      acc0 = mfma(v4u{a00[0], a00[1], a01[0], a01[1]}, pf, acc0);
      acc1 = mfma(v4u{a10[0], a10[1], a11[0], a11[1]}, pf, acc1);
    }
    {
      const v4u v = __builtin_bit_cast(v4u, n < 4 ? acc0 : acc1);
      ST128((v4u{nosent(v[0]), nosent(v[1]), nosent(v[2]), nosent(v[3])}), rt, po, 0, AUX_SC1);
    }
    return;
  }

  if (w < 8) {
    unsigned char* a_ws = p.ws; const u16 *a_w = isQ ? p.wq : p.wmod, *a_i = isQ ? p.x : p.c;
    u64 tstart64 = __builtin_amdgcn_s_memtime();
    __builtin_amdgcn_sched_barrier(0);
    const int nact = isH ? 8 / FH : 8;
    const int hl = bid & 15, hf = bid >> 4;
    int pitch = isQ ? 8192 : 6144;
    const int row0 = (isH ? hf * (512 / FH) : 0) + w * 64;
    int so = row0 * pitch + (isH ? hl : isQ ? bid - Q0 : bid - M0 + 16) * 128, vo = g * 8 * pitch + n * 8;
    int io = isQ ? lane * 16 : (n & 1) * 1024 + row0 * 2 + g * 16, i1o = isQ ? 1024 : 64;
    asm volatile("" : "+s"(pitch), "+s"(so), "+s"(i1o)); asm volatile("" : "+v"(vo), "+v"(io));
    asm volatile("" : "+s"(tstart64), "+s"(a_ws), "+s"(a_w), "+s"(a_i));
    ARGS_READY();
    const unsigned tstart = (unsigned)tstart64;
    const rsrc_t rw = mkrs(a_ws, W_END);
    unsigned ep = LD32(rw, 0, W_EPOCH, AUX_SC1);
    if (bid < JEND && w < nact) {
      const rsrc_t rj = mkrs(a_w, 512 * pitch), ri = mkrs(a_i, 2048);
      const v4u i0 = LD128(ri, io, 0, 0), i1 = LD128(ri, io, i1o, 0);
      unsigned xd0 = __builtin_amdgcn_raw_buffer_load_b16(ri, lane * 2, w * 128, 0), xd1 = __builtin_amdgcn_raw_buffer_load_b16(ri, lane * 2, 1024 + w * 128, 0);
      v2u wr[16];
#pragma unroll
      for (int i = 0; i < 16; ++i) wr[i] = LD64(rj, vo, so + ((i >> 3) * 32 + (i & 7)) * pitch, AUX_NT);
      __builtin_amdgcn_sched_barrier(0);
      v4u a0, a1;
      float ln0 = 0.f, ln1 = 0.f;
      if (!isQ) {
#pragma unroll
        for (int k = 0; k < 4; ++k) { a0[k] = cvtpk(silu_x(lo2f(i0[k])), silu_x(hi2f(i0[k]))); a1[k] = cvtpk(silu_x(lo2f(i1[k])), silu_x(hi2f(i1[k]))); }
      } else {
        a0 = i0; a1 = i1;
        asm volatile("" : "+v"(xd0), "+v"(xd1));
        float sm0 = 0.f, sm1 = 0.f;
#pragma unroll
        for (int k = 0; k < 4; ++k) { sm0 += lo2f(i0[k]) + hi2f(i0[k]); sm1 += lo2f(i1[k]) + hi2f(i1[k]); }
        const float mean0 = wave_sum(sm0) * (1.f / 512.f), mean1 = wave_sum(sm1) * (1.f / 512.f);
        float q0 = 0.f, q1 = 0.f;
#pragma unroll
        for (int k = 0; k < 4; ++k) {
          float t = lo2f(i0[k]) - mean0; q0 += t * t; t = hi2f(i0[k]) - mean0; q0 += t * t;
          t = lo2f(i1[k]) - mean1; q1 += t * t; t = hi2f(i1[k]) - mean1; q1 += t * t;
        }
        const float isd0 = rcp(rbf(__builtin_amdgcn_sqrtf(rbf(rbf(wave_sum(q0) * (1.f / 512.f)) + 1e-5f))));
        const float isd1 = rcp(rbf(__builtin_amdgcn_sqrtf(rbf(rbf(wave_sum(q1) * (1.f / 512.f)) + 1e-5f))));
        ln0 = rbf(rbf(lo2f(xd0) - rbf(mean0)) * isd0); ln1 = rbf(rbf(lo2f(xd1) - rbf(mean1)) * isd1);
      }
      asm volatile("" : "+v"(a0), "+v"(a1), "+v"(ln0), "+v"(ln1));
      v4u bj[8];
#pragma unroll
      for (int s = 0; s < 2; ++s)
#pragma unroll
        for (int d = 0; d < 4; ++d)
#pragma unroll
          for (int t = 0; t < 4; ++t)
            bj[s * 4 + t][d] = (t & 1) ? pk_hi(wr[s * 8 + 2 * d][t >> 1], wr[s * 8 + 2 * d + 1][t >> 1]) : pk_lo(wr[s * 8 + 2 * d][t >> 1], wr[s * 8 + 2 * d + 1][t >> 1]);
      if (isQ) {
        asm volatile("" : "+v"(bj[0]), "+v"(bj[1]), "+v"(bj[2]), "+v"(bj[3]), "+v"(bj[4]), "+v"(bj[5]), "+v"(bj[6]), "+v"(bj[7]));
        asm volatile("" : "+v"(ep));
        const unsigned tag = ep + 1u;
        v4u t[2 * FH];
        for (unsigned spins = 0; (unsigned)__builtin_amdgcn_s_memtime() - tstart < (unsigned)(HT * CLKM) && ++spins < 4096u;) __builtin_amdgcn_s_sleep(1);
#pragma unroll 1
        for (unsigned spins = 0;;) {
          bool ok = true;
#pragma unroll
          for (int k = 0; k < 2 * FH; ++k) t[k] = LD128(rw, lane * 16 + k * 1024, W_HG + w * (2048 * FH), AUX_SC1);
#pragma unroll
          for (int k = 0; k < 2 * FH; ++k) ok &= (t[k][1] == tag) & (t[k][3] == tag);
          if (__all(ok) || ++spins > SPIN_G) break;
          __builtin_amdgcn_s_sleep(8); asm volatile("" ::: "memory");
        }
        float sa0 = 0.f, sa1 = 0.f, ca0 = 0.f, ca1 = 0.f;
#pragma unroll
        for (int k = 0; k < FH; ++k) { sa0 += __uint_as_float(t[k][0]); sa1 += __uint_as_float(t[k][2]); ca0 += __uint_as_float(t[FH + k][0]); ca1 += __uint_as_float(t[FH + k][2]); }
        const unsigned hh = cvtpk(rbf(ln0 * rbf(1.f + rbf(ca0))) + rbf(sa0), rbf(ln1 * rbf(1.f + rbf(ca1))) + rbf(sa1));
        *(LAS u16*)(L + L_HX + w * 256 + lane * 2) = (u16)hh; *(LAS u16*)(L + L_HX + w * 256 + 128 + lane * 2) = (u16)(hh >> 16);
        asm volatile("" ::: "memory");
        a0 = *(LAS v4u*)(L + L_HX + w * 256 + (n & 1) * 128 + g * 16); a1 = *(LAS v4u*)(L + L_HX + w * 256 + (n & 1) * 128 + 64 + g * 16);
      }
      f32x4 y[4];
#pragma unroll
      for (int t = 0; t < 4; ++t) y[t] = mfma(a1, bj[4 + t], mfma(a0, bj[t], f32x4{0.f, 0.f, 0.f, 0.f}));
      if (lane < 32) *(LAS f32x4*)(L + L_RED + w * 512 + lane * 16) = g ? f32x4{y[0][1], y[1][1], y[2][1], y[3][1]} : f32x4{y[0][0], y[1][0], y[2][0], y[3][0]};
      asm volatile("s_waitcnt lgkmcnt(0)" ::: "memory");
      unsigned old = 0;
      if (lane == 0) old = __hip_atomic_fetch_add((LAS unsigned*)(L + L_JCNT), 1u, __ATOMIC_RELAXED, __HIP_MEMORY_SCOPE_WORKGROUP);
      if (__builtin_amdgcn_readfirstlane(old) == (unsigned)(nact - 1)) {
        asm volatile("" : "+v"(ep) :: "memory");
        const unsigned tag = ep + 1u;
        f32x2 s = {0.f, 0.f};
#pragma unroll
        for (int ww = 0; ww < 8 / FH; ++ww) s += *(LAS f32x2*)(L + L_RED + ww * 512 + lane * 8);
        if (isH) {
          GAS u64* o = (GAS u64*)(a_ws + W_HG + (hl & 7) * (2048 * FH) + ((hl >> 3) * FH + hf) * 1024 + (lane & 31) * 32 + (lane >> 5) * 8);
          __hip_atomic_store(o, ((u64)tag << 32) | __float_as_uint(s[0]), RLX_AGENT);
          __hip_atomic_store(o + 2, ((u64)tag << 32) | __float_as_uint(s[1]), RLX_AGENT);
        } else {
#pragma unroll
          for (int ww = 8 / FH; ww < 8; ++ww) s += *(LAS f32x2*)(L + L_RED + ww * 512 + lane * 8);
          const int o = (isQ ? W_QG + (lane >> 5) * 16384 + (bid - Q0) * 256 : W_MG + (lane >> 5) * 8192 + (bid - M0) * 256) + (lane & 31) * 8;
          __hip_atomic_store((GAS u64*)(a_ws + o), ((u64)tag << 32) | cvtpk(s[0], s[1]), RLX_AGENT);
        }
      }
    }
    asm volatile("" : "+v"(ep) :: "memory");
    const unsigned tag = ep + 1u;
    if (lane == 0) __hip_atomic_fetch_add((LAS unsigned*)(L + L_ECNT), 1u, __ATOMIC_RELAXED, __HIP_MEMORY_SCOPE_WORKGROUP);
    if (w == 7) {
      for (unsigned spins = 0; (unsigned)__builtin_amdgcn_s_memtime() - tstart < (unsigned)(QT * CLKM) && ++spins < 4096u;) __builtin_amdgcn_s_sleep(1);
      v4u t0, t1;
#pragma unroll 1
      for (unsigned spins = 0;;) {
        t0 = LD128(rw, lane * 16, W_QG + b * 16384 + kvh * 2048, AUX_SC1); t1 = LD128(rw, lane * 16 + 1024, W_QG + b * 16384 + kvh * 2048, AUX_SC1);
        if (__all((t0[1] == tag) & (t0[3] == tag) & (t1[1] == tag) & (t1[3] == tag)) || ++spins > SPIN_G) break;
        __builtin_amdgcn_s_sleep(8); asm volatile("" ::: "memory");
      }
      *(LAS v2u*)(L + L_Q + lane * 8) = v2u{t0[0], t0[2]}; *(LAS v2u*)(L + L_Q + 512 + lane * 8) = v2u{t1[0], t1[2]};
      asm volatile("s_waitcnt lgkmcnt(0)" ::: "memory");
      *(volatile LAS unsigned*)(L + L_QRDY) = 1u;
    }
    if (w == 6) {
      const v4u s4 = {SENT, SENT, SENT, SENT};
      lds_wait<8>(L, L_QRDY, 1);
      ST128(s4, rw, lane * 16, W_PO + bid * 2048, AUX_SC1);
      ST128(s4, rw, lane * 16 + 1024, W_PO + bid * 2048, AUX_SC1);
      const int oo = hasC ? W_OP + (b * 16 * 56 + fblk) * 128 : (int)OOB;
      ST128(s4, rw, r8 * (56 * 128) + c8 * 16, oo, AUX_SC1);
      ST128(s4, rw, (8 + r8) * (56 * 128) + c8 * 16, oo, AUX_SC1);
      ST128(s4, rw, lane < 4 ? W_ML + bid * 128 + lane * 16 : lane == 4 ? W_HU + bid * 128 : (g == 1 && isA) ? W_AB + (agrp * 512 + aln * 64) * 4 + n * 16 : (int)OOB, 0, AUX_SC1);
      waitvm<0>();
      *(volatile LAS unsigned*)(L + L_CLRD) = 1u;
      lds_wait<2>(L, L_ECNT, 9);
      if (lane == 0) __hip_atomic_store((GAS u64*)(a_ws + W_CL + bid * 8), ((u64)tag << 32) | (tag ^ 0x5bd1e995u), RLX_AGENT);
    }
    v4u bw[8];
    auto wslot = [&](int i) { lds_wait<2>(L, L_FULL, WBASE + (6 * w + i) * WSP); };
    if (isA) {
      const rsrc_t ro = mkrs(p.wo, 4096 * 512 * 2);
      v2u r[16];
#pragma unroll
      for (int i = 0; i < 16; ++i) { if ((i & 3) == 0) wslot((i >> 2) + (i >> 3)); r[i] = LD64(ro, g * 1024 + n * 8, (akv * 512 + w * 64 + 4 * i) * 1024 + aln * 128, 0); }
#pragma unroll
      for (int a = 0; a < 8; ++a)
#pragma unroll
        for (int t = 0; t < 4; ++t) bw[(a >> 2) * 4 + t][a & 3] = (t & 1) ? pk_hi(r[2 * a][t >> 1], r[2 * a + 1][t >> 1]) : pk_lo(r[2 * a][t >> 1], r[2 * a + 1][t >> 1]);
    } else {
      const rsrc_t r1 = mkrs(p.w1, hasC ? 512 * 1792 * 2 : 0), r2 = mkrs(p.w2, hasC ? 1792 * 512 * 2 : 0);
      unsigned ra[16]; v2u rb[8];
#pragma unroll
      for (int i = 0; i < 16; ++i) { if ((i & 3) == 0) wslot(i >> 2); ra[i] = LD32(r1, g * 3584 + n * 4, (w * 64 + 4 * i) * 3584 + fblk * 64, 0); }
#pragma unroll
      for (int i = 0; i < 8; ++i) { if ((i & 3) == 0) wslot(4 + (i >> 2)); rb[i] = LD64(r2, g * 8192 + n * 8, fblk * 32768 + i * 1024 + w * 128, 0); }
#pragma unroll
      for (int a = 0; a < 8; ++a) { bw[(a >> 2) * 2][a & 3] = pk_lo(ra[2 * a], ra[2 * a + 1]); bw[(a >> 2) * 2 + 1][a & 3] = pk_hi(ra[2 * a], ra[2 * a + 1]); }
#pragma unroll
      for (int d = 0; d < 4; ++d)
#pragma unroll
        for (int t = 0; t < 4; ++t) bw[4 + t][d] = (t & 1) ? pk_hi(rb[2 * d][t >> 1], rb[2 * d + 1][t >> 1]) : pk_lo(rb[2 * d][t >> 1], rb[2 * d + 1][t >> 1]);
    }
    asm volatile("" : "+v"(bw[0]), "+v"(bw[1]), "+v"(bw[2]), "+v"(bw[3]), "+v"(bw[4]), "+v"(bw[5]), "+v"(bw[6]), "+v"(bw[7]));
    const int col = w * 64 + 4 * n + g;
    const unsigned xin = __builtin_amdgcn_raw_buffer_load_b16(mkrs(p.x, 2048), col * 2, b * 1024, 0);
    lds_wait<8>(L, L_FULL, 320 + 8 * w);
    v2u mg[4];
    {
      const rsrc_t rm = mkrs(a_ws, isA ? 0 : W_END);
#pragma unroll 1
      for (unsigned spins = 0;;) {
        bool ok = true;
#pragma unroll
        for (int k = 0; k < 3; ++k) mg[k] = LD64(rm, (col >> 1) * 8 + k * 2048, W_MG + b * 8192, AUX_SC1);
        mg[3] = LD64(rm, g * 8, W_MG + b * 8192 + 6144 + ocol * 4, AUX_SC1);
        const v2u e = LD64(rw, (lane & 31) * 8, W_CL + w * 256, AUX_SC1);
#pragma unroll
        for (int k = 0; k < 4; ++k) ok &= isA | (mg[k][1] == tag);
        ok &= (e[0] == (tag ^ 0x5bd1e995u)) & (e[1] == tag);
        if (__all(ok) || ++spins > SPIN_G) break;
        __builtin_amdgcn_s_sleep(32); asm volatile("" ::: "memory");
      }
    }
    if (lane == 0) __hip_atomic_fetch_add((LAS unsigned*)(L + L_VCNT), 1u, __ATOMIC_RELAXED, __HIP_MEMORY_SCOPE_WORKGROUP);
    float xf = lo2f(xin), ga = (g & 1) ? hi2f(mg[0][0]) : lo2f(mg[0][0]), sb = (g & 1) ? hi2f(mg[1][0]) : lo2f(mg[1][0]);
    float cb1 = rbf(1.f + ((g & 1) ? hi2f(mg[2][0]) : lo2f(mg[2][0]))), gv0 = lo2f(mg[3][0]), gv1 = hi2f(mg[3][0]);
    GAS unsigned* outp = (GAS unsigned*)(p.out + b * 512 + ocol + 2 * g);
    asm volatile("" : "+v"(xf), "+v"(ga), "+v"(sb), "+v"(cb1), "+v"(gv0), "+v"(gv1), "+v"(outp));

    {
      unsigned spins = 0;
      float w0 = 0.f, w1 = 0.f, w2 = 0.f, w3 = 0.f;
      lds_wait<8>(L, L_VCNT, 8);
      if (bid == 0 && w == 1 && lane == 0) __hip_atomic_store((GAS unsigned*)(a_ws + W_EPOCH), tag, RLX_AGENT);
      lds_wait<8>(L, L_FULL, 704 + 8 * w);
      if (isA) {
        v2u ml;
#pragma unroll 1
        for (;;) {
          ml = LD64(rw, (n ^ g) * 128, W_ML + agrp * 2048 + (w >> 1) * 16, AUX_SC1);
          if (!__any(max(ml[0], ml[1]) == SENT) || ++spins > SPIN_G) break;
          __builtin_amdgcn_s_sleep(16); asm volatile("" ::: "memory");
        }
        const float mi = __uint_as_float(ml[0]);
        float M = fmaxf(mi, dpp<0x128>(mi)); M = fmaxf(M, dpp<0x124>(M)); M = fmaxf(M, dpp<0x122>(M)); M = fmaxf(M, dpp<0x121>(M));
        const float e = __builtin_amdgcn_exp2f((mi - M) * L2E);
        const float wg = e * rcp(row_sum(e * __uint_as_float(ml[1])));
        w0 = dpp<0x150>(wg); w1 = dpp<0x154>(wg); w2 = dpp<0x158>(wg); w3 = dpp<0x15C>(wg);
      }
      int gv = g * 2048 + n * 16, gs = (isA ? W_PO + agrp * 32768 : W_AB + b * 16384) + w * 256;
      int l0 = isA ? (lane < 16 ? L_RAB + w * 256 + n * 16 : L_NUL) : L_ST + w * 16 + (lane >> 5) * 8, l1 = L_X1 + col * 4, l2 = L_ST + c8 * 16;
      int l3 = lane < 16 ? L_RC + n * 64 + w * 4 : L_NUL, l4 = (isA ? L_RAB : L_RC) + lane * 16, l5 = L_X1 + ocol * 4 + g * 8;
      int pv_ = isA ? n * 16 + g * 4 : (lane < 16 && hasC) ? (n >> 3) * (56 * 128) + (n & 7) * 16 : (int)OOB;
      int ps = isA ? W_AB + (agrp * 512 + aln * 64) * 4 : W_OP + ((b * 16 + 2 * w) * 56 + fblk) * 128;
      int dv = lane < 56 ? lane * 128 : (int)OOB, ds = W_OP + (b * 16 + (ocol >> 5)) * (56 * 128) + (ocol & 31) * 4;
      asm volatile("" : "+v"(w0), "+v"(w1), "+v"(w2), "+v"(w3), "+v"(gv), "+v"(l0), "+v"(l1), "+v"(l2), "+v"(l3), "+v"(l4), "+v"(l5), "+v"(pv_), "+v"(dv));
      asm volatile("" : "+s"(gs), "+s"(ps), "+s"(ds));
      auto pace = [&](int delay) {
        const unsigned ta = (unsigned)__builtin_amdgcn_s_memrealtime(), ca = (unsigned)__builtin_amdgcn_s_memtime();
        asm volatile("" :: "s"(ta), "s"(ca));
        lds_wait<8>(L, L_FULL, 16 * NUNIT - 64); lds_wait<1>(L, L_FULL, 16 * NUNIT);
        const int hs = W_HU + (b * 8 + w) * 2048;
        unsigned hu = LD32(rw, n * 128, hs, AUX_SC1);
        const unsigned tn = (unsigned)__builtin_amdgcn_s_memrealtime(), c0 = (unsigned)__builtin_amdgcn_s_memtime();
        float cpt = (float)(c0 - ca) * rcp((float)(tn - ta));
        int t0 = L_TS + w * 64 + n * 4, t1 = L_TS + lane * 8;
        asm volatile("" : "+v"(cpt), "+v"(t0), "+v"(t1));
#pragma unroll 1
        for (;;) {
          const bool badh = hu == SENT;
          if (!__any(badh) || ++spins > SPIN_G) break;
          __builtin_amdgcn_s_sleep(HSL);
          asm volatile("" ::: "memory");
          if (badh) hu = LD32(rw, n * 128, hs, AUX_SC1);
        }
        *(LAS unsigned*)LP(t0) = hu;
        bar();
        const v2u ts = *(LAS v2u*)LP(t1);
        const int due = (int)((float)(max((int)(ts[0] - tn), (int)(ts[1] - tn)) + delay) * cpt);
        for (unsigned k = 0; __any((int)((unsigned)__builtin_amdgcn_s_memtime() - c0) < due) && ++k < 512u;) {}
        asm volatile("" ::: "memory");
      };
      const f32x4 z = {0.f, 0.f, 0.f, 0.f};
      if (isA) {
        v4u o4[4];
        pace(DA);
#pragma unroll 1
        do {
          asm volatile("" ::: "memory");
#pragma unroll
          for (int i = 0; i < 4; ++i) o4[i] = LD128(rw, gv, gs + i * 8192, AUX_SC1);
        } while (__any(max(max(umax4(o4[0]), umax4(o4[1])), max(umax4(o4[2]), umax4(o4[3]))) == SENT) && ++spins < SPIN_G);
        const f32x4 o = (w0 * __builtin_bit_cast(f32x4, o4[0]) + w1 * __builtin_bit_cast(f32x4, o4[1])) + (w2 * __builtin_bit_cast(f32x4, o4[2]) + w3 * __builtin_bit_cast(f32x4, o4[3]));
        v4u a0, a1; afrag(tr4(o), a0, a1);
        f32x4 y[4];
#pragma unroll
        for (int t = 0; t < 4; ++t) y[t] = mfma(a0, bw[t], z);
        __builtin_amdgcn_sched_barrier(0);
#pragma unroll
        for (int t = 0; t < 4; ++t) y[t] = mfma(a1, bw[4 + t], y[t]);
        *(LAS f32x4*)LP(l0) = f32x4{y[0][0], y[1][0], y[2][0], y[3][0]};
        bar();
        if (w == 0) {
          const float t = tr4(*(LAS f32x4*)LP(l4) + *(LAS f32x4*)(LP(l4) + 1024));
          __builtin_amdgcn_raw_buffer_store_b32(nosent(__float_as_uint(t)), rw, pv_, ps, AUX_SC1);
        }
      } else {
        v4u a[2];
        pace(DC);
#pragma unroll 1
        do {
          asm volatile("" ::: "memory");
#pragma unroll
          for (int i = 0; i < 2; ++i) a[i] = LD128(rw, gv, gs + i * 8192, AUX_SC1);
        } while (__any(max(umax4(a[0]), umax4(a[1])) == SENT) && ++spins < SPIN_G);
        const float x1 = rbf(xf + rbf(ga * rbf(tr4(__builtin_bit_cast(f32x4, a[0]) + __builtin_bit_cast(f32x4, a[1])))));
        *(LAS float*)LP(l1) = x1;
        const float pv = __builtin_bit_cast(float, __builtin_amdgcn_readfirstlane(__builtin_bit_cast(int, x1)));
        const float dx = x1 - pv;
        float ss = tr32(dx, dx * dx); ss = row_sum(tr16(ss, ss));
        *(LAS f32x2*)LP(l0) = f32x2{ss, pv};
        bar();
        const f32x4 st = *(LAS f32x4*)LP(l2);
        const float mw = st[1] + st[0] * (1.f / 64.f), c0 = dpp<0x150>(mw), dm = mw - c0;
        float t1 = dm, t2 = (st[2] - st[0] * st[0] * (1.f / 64.f)) + 64.f * dm * dm;
        t1 += dpp<0xB1>(t1); t2 += dpp<0xB1>(t2); t1 += dpp<0x4E>(t1); t2 += dpp<0x4E>(t2); t1 += dpp<0x141>(t1); t2 += dpp<0x141>(t2);
        t1 *= 0.125f;
        const float mean = rbf(c0 + t1), var = rbf(fmaxf(t2 * (1.f / 512.f) - t1 * t1, 0.f));
        const float isd = rcp(rbf(__builtin_amdgcn_sqrtf(rbf(var + 1e-5f))));
        v4u a0, a1; afrag(rbf(rbf(rbf(x1 - mean) * isd) * cb1) + sb, a0, a1);
        const f32x4 yp = mfma(a0, bw[0], z), yq = mfma(a0, bw[1], z);
        __builtin_amdgcn_sched_barrier(0);
        const float y0 = mfma(a1, bw[2], yp)[0], y1 = mfma(a1, bw[3], yq)[0];
        *(LAS float*)LP(l3) = y0; *(LAS float*)(LP(l3) + 32) = y1;
        bar();
        const f32x4 s = *(LAS f32x4*)LP(l4);
        float t0 = (s[0] + s[2]) + (s[1] + s[3]);
        t0 += dpp<0xB1>(t0);
        const float hv = silu_f(rbf(t0));
        const unsigned P = cvtpk(hv, dpp<0x102>(hv));
        const v4u ah = {P, dppu<0x104>(P), dppu<0x108>(P), dppu<0x10C>(P)};
        f32x4 q0 = mfma(ah, bw[4], z), q1 = mfma(ah, bw[5], z), q2 = mfma(ah, bw[6], z), q3 = mfma(ah, bw[7], z);
        asm volatile("" : "+v"(q0), "+v"(q1), "+v"(q2), "+v"(q3));
        ST128((v4u{nosent(__float_as_uint(q0[0])), nosent(__float_as_uint(q1[0])), nosent(__float_as_uint(q2[0])), nosent(__float_as_uint(q3[0]))}), rw, pv_, ps, AUX_SC1);
        if (w == 0) {
          const f32x2 xv = *(LAS f32x2*)LP(l5);
          __builtin_amdgcn_s_sleep(DPRE);
          v4u d0, d1;
#pragma unroll 1
          do {
            asm volatile("" ::: "memory");
            d0 = LD128(rw, dv, ds, AUX_SC1); d1 = LD128(rw, dv + 16, ds, AUX_SC1);
          } while (__any(max(umax4(d0), umax4(d1)) == SENT) && ++spins < SPIN_G);
          const f32x4 e0 = __builtin_bit_cast(f32x4, d0), e1 = __builtin_bit_cast(f32x4, d1);
          const float ya = row_sum(tr16(tr32(e0[0], e1[0]), tr32(e0[2], e1[2]))), yb = row_sum(tr16(tr32(e0[1], e1[1]), tr32(e0[3], e1[3])));
          *outp = cvtpk(xv[0] + rbf(gv0 * rbf(ya)), xv[1] + rbf(gv1 * rbf(yb)));
        }
      }
    }
    return;
  }

  {
    const u16 *a_k = p.K, *a_v = p.V;
    __builtin_amdgcn_sched_barrier(0);
    const int o0 = g * 256 + ((n ^ (2 * g)) * 16), o1 = o0 ^ 128, o2 = o0 ^ 16, o3 = o0 ^ 144;
    int deep = (!isH && bid < JEND) ? QCATCH : 0;
    asm volatile("" : "+s"(deep));
    u64 kb = (u64)(a_k + (size_t)grp * (16 * NUNIT * 4096)), vb = (u64)(a_v + (size_t)grp * (16 * NUNIT * 4096));
    asm volatile("" : "+s"(kb), "+s"(vb));
    ARGS_READY();
    unsigned freed = 0; int pub = 0;
    unsigned adv = 0;
#pragma unroll 1
    for (int h = 0; h < 2 * NUNIT; ++h) {
      const int ua = unit_addr(h, grp, mem) * 8192;
      if ((int)freed < h - (NSLOT - 1)) {
#pragma unroll 1
        for (unsigned spins = 0;;) {
          const v4u f = *(volatile LAS v4u*)(L + L_FREE);
          freed = __builtin_amdgcn_readfirstlane(min(min(f[0], f[1]), min(f[2], f[3])));
          if ((int)freed >= h - (NSLOT - 1)) break;
          if (++spins > SPIN_L) { freed = 1u << 30; break; }
        }
      }
      const unsigned lb = (h & (NSLOT - 1)) * 8192;
      const rsrc_t rs = mkrs((const void*)kb, 16 * NUNIT * 8192);
      deep = max(deep, (int)__builtin_amdgcn_readfirstlane(adv));
      auto half = [&](auto dc) {
        constexpr int W = decltype(dc)::value - 4;
        waitvm<W>(); dma4(rs, o0, o1, o2, o3, ua, lb); if (!((W + 3) & 4)) { pub = max(pub, 8 * h - W); *FULL = pub; }
        adv = *(volatile LAS unsigned*)(L + L_ADV);
        waitvm<W>(); dma4(rs, o0, o1, o2, o3, ua + 4096, lb + 4096); if (((W + 3) & 4)) { pub = max(pub, 8 * h + 4 - W); *FULL = pub; }
      };
      if (h < deep && h < 128 - NORM) half(IC<DDEEP>{}); else half(IC<DEPTH>{});
      if (h == 63) kb = vb;
    }
    waitvm<24>(); pub = max(pub, 16 * NUNIT - 24); *FULL = pub;
    waitvm<16>(); pub = max(pub, 16 * NUNIT - 16); *FULL = pub;
    waitvm<8>(); *FULL = 16 * NUNIT - 8;
    waitvm<0>(); *FULL = 16 * NUNIT;
  }
}

#undef p
extern "C" void kernel_launch(void* const* d_in, const int* in_sizes, int n_in, void* d_out, int out_size, void* d_ws, size_t ws_size, hipStream_t stream) {
  Params p;
  p.x = (const u16*)d_in[0]; p.K = (const u16*)d_in[1]; p.V = (const u16*)d_in[2]; p.wq = (const u16*)d_in[3]; p.wo = (const u16*)d_in[4];
  p.w1 = (const u16*)d_in[6]; p.w2 = (const u16*)d_in[7]; p.c = (const u16*)d_in[8]; p.wmod = (const u16*)d_in[9];
  p.out = (u16*)d_out; p.ws = (unsigned char*)d_ws;
  (void)hipFuncSetAttribute((const void*)adaln_block_kernel, hipFuncAttributeMaxDynamicSharedMemorySize, L_END);
  hipLaunchKernelGGL(adaln_block_kernel, dim3(NWG), dim3(NTHR), L_END, stream, p);
}
```
